```python
import torch

def _softmax(x, axis=-1):
    return torch.softmax(x, dim=axis)
def _gelu(x):
    return torch.nn.functional.gelu(x, approximate="tanh")
def _silu(x):
    return torch.nn.functional.silu(x)
def _sigmoid(x):
    return torch.sigmoid(x)
def _layernorm(x, axis=-1, eps=1e-5):
    m = x.mean(dim=axis, keepdim=True)
    v = x.var(dim=axis, keepdim=True, unbiased=False)
    return (x - m) / torch.sqrt(v + eps)
def _scatter_add(tgt, idx, src, axis=0):
    return tgt.clone().index_add_(axis, idx, src)
def _segment_sum(src, seg, n):
    out = torch.zeros((n,) + src.shape[1:], dtype=src.dtype)
    return out.index_add_(0, seg, src)
def _pad(x, pw):
    flat = tuple(v for pair in reversed(pw) for v in pair)
    return torch.nn.functional.pad(x, flat)

kernel_name = 'sequential_preLN__softmax_full_mha__geglu_ffn__prompt'

def setup_inputs():

    _w = (lambda a: (lambda t: t.to(torch.bfloat16) if t.is_floating_point() else t)(torch.as_tensor(a)))
    def _A_setup():
        B, S, D = 1, 2048, 512
        return {
            "x": _w((lambda *s: torch.randn(*s, dtype=torch.bfloat16) * 0.05)(B, S, D)),
            "wqkv": _w((lambda *s: torch.randn(*s, dtype=torch.bfloat16) * 0.05)(D, 3 * D)),
            "wo": _w((lambda *s: torch.randn(*s, dtype=torch.bfloat16) * 0.05)(D, D)),
        }
    _A_in = _A_setup()

    def _B_setup():
        B, S, D, F = 1, 2048, 512, 1792
        return {
            "x": _w((lambda *s: torch.randn(*s, dtype=torch.bfloat16) * 0.05)(B, S, D)),
            "w_gate": _w((lambda *s: torch.randn(*s, dtype=torch.bfloat16) * 0.05)(D, F)),
            "w_up": _w((lambda *s: torch.randn(*s, dtype=torch.bfloat16) * 0.05)(D, F)),
            "w_down": _w((lambda *s: torch.randn(*s, dtype=torch.bfloat16) * 0.05)(F, D)),
        }
    _B_in = _B_setup()

    return {
        "A_x": _A_in["x"],
        "A_wqkv": _A_in["wqkv"],
        "A_wo": _A_in["wo"],
        "B_x": _B_in["x"],
        "B_w_gate": _B_in["w_gate"],
        "B_w_up": _B_in["w_up"],
        "B_w_down": _B_in["w_down"],
    }

def reference(A_x, A_wqkv, A_wo, B_x, B_w_gate, B_w_up, B_w_down):
    def _A_ref(x, wqkv, wo):
        B, S, D = x.shape
        H = 32; Dh = D // H
        qkv = (lambda x, *d: x.permute(*d))((x @ wqkv).reshape(B, S, 3, H, Dh), 2, 0, 3, 1, 4)
        q, k, v = qkv[0], qkv[1], qkv[2]
        scale = Dh ** -0.5
        scores = torch.einsum("bhqd,bhkd->bhqk", q, k) * scale
        p = _softmax(scores, axis=-1)
        o = torch.einsum("bhqk,bhkd->bhqd", p, v)
        return (lambda x, *d: x.permute(*d))(o, 0, 2, 1, 3).reshape(B, S, D) @ wo

    def _B_ref(x, w_gate, w_up, w_down):
        return (_gelu(x @ w_gate) * (x @ w_up)) @ w_down

    x = A_x
    h = x + _A_ref(_layernorm(x, axis=-1), A_wqkv, A_wo)
    h2 = h + _B_ref(_layernorm(h, axis=-1), B_w_gate, B_w_up, B_w_down)
    return h2
```

```cpp
#include <hip/hip_runtime.h>
#include <hip/hip_bf16.h>
#include <stdint.h>

typedef __attribute__((ext_vector_type(8))) short bf16x8;
typedef __attribute__((ext_vector_type(4))) short s16x4;
typedef __attribute__((ext_vector_type(16))) float f32x16;
typedef __attribute__((ext_vector_type(4))) float f32x4;
typedef __attribute__((ext_vector_type(4))) unsigned u32x4;
typedef __attribute__((ext_vector_type(2))) unsigned u32x2;
typedef unsigned short u16;
#define LAS __attribute__((address_space(3)))
typedef LAS const char* lds_cptr;
typedef LAS char* lds_ptr;

constexpr int S_ = 2048, D_ = 512, NH = 32, F_ = 1792;
constexpr float LN_EPS = 1e-5f;
constexpr float QSCALE = 0.25f * 1.4426950408889634f;

constexpr size_t WS_Q = 0;
constexpr size_t WS_K = WS_Q + (size_t)NH * 64 * 1024;
constexpr int VBLK = 528;
constexpr int VHEAD = 128 * VBLK;
constexpr size_t WS_V = WS_K + (size_t)NH * 64 * 1024;
constexpr size_t WS_O = WS_V + (size_t)NH * VHEAD;
constexpr size_t WS_H = WS_O + (size_t)64 * 32 * 1024;
constexpr size_t WS_A = WS_H + (size_t)64 * 32 * 1024;
constexpr size_t WS_CS = WS_A + (size_t)64 * 112 * 1024;

#define MFMA32(a, b, c) __builtin_amdgcn_mfma_f32_32x32x16_bf16(a, b, c, 0, 0, 0)
#define WAITV(n) asm volatile("s_waitcnt vmcnt(" #n ")" ::: "memory")
#define BARRIER() asm volatile("s_waitcnt lgkmcnt(0)\n\ts_barrier" ::: "memory")

__device__ __forceinline__ void glds16(const void* sbase, unsigned voff, unsigned lds_base) {
    unsigned sv;
    asm volatile("s_mov_b32 %0, m0\n\ts_mov_b32 m0, %3\n\ts_nop 0\n\tglobal_load_lds_dwordx4 %1, %2\n\ts_mov_b32 m0, %0"
                 : "=&s"(sv) : "v"(voff), "s"(sbase), "s"(lds_base) : "memory");
}
typedef __attribute__((ext_vector_type(2))) float f32x2;
typedef __attribute__((ext_vector_type(2))) __bf16 bf16x2;
__device__ __forceinline__ unsigned cvtpk(float lo, float hi) {
    return __builtin_bit_cast(unsigned, __builtin_convertvector((f32x2){lo, hi}, bf16x2));
}
__device__ __forceinline__ bf16x8 lds_b128(lds_cptr p) { return *(const LAS bf16x8*)p; }
__device__ __forceinline__ s16x4 lds_tr(lds_cptr p) {
    return __builtin_amdgcn_ds_read_tr16_b64_v4i16((LAS s16x4*)p);
}
__device__ __forceinline__ bf16x8 cat8(s16x4 a, s16x4 b) {
    return (bf16x8){a[0], a[1], a[2], a[3], b[0], b[1], b[2], b[3]};
}
__device__ __forceinline__ float dot2bf(unsigned a, unsigned b, float c) {
    return __builtin_amdgcn_fdot2_f32_bf16(__builtin_bit_cast(bf16x2, a), __builtin_bit_cast(bf16x2, b), c, false);
}
__device__ __forceinline__ float other_half(float v) {
    return __shfl_xor(v, 32);
}
__device__ __forceinline__ float bf_lo(unsigned u) { return __uint_as_float(u << 16); }
__device__ __forceinline__ float bf_hi(unsigned u) { return __uint_as_float(u & 0xffff0000u); }
__device__ __forceinline__ int uni(int v) { return __builtin_amdgcn_readfirstlane(v); }

constexpr int QK_SLOT = 28 * 1024, QK_NSLOT = 5, QK_STAT = QK_SLOT * QK_NSLOT, QK_LDS = QK_STAT + 4 * 160 * 4;

__global__ void __launch_bounds__(256, 1)
k_qkv(const u16* __restrict__ x, const u16* __restrict__ w, char* __restrict__ ws) {
    extern __shared__ __attribute__((aligned(16))) char smem[];
    const int tid = threadIdx.x, lane = tid & 63, wid = uni(tid >> 6), r = lane & 31, hh = lane >> 5;
    const int b = blockIdx.x, xcd = b & 7, ii = b >> 3;
    const int rb = (xcd >> 1) * 4 + (ii >> 3), hp = (xcd & 1) * 8 + (ii & 7);
    const unsigned lds0 = (unsigned)(uintptr_t)smem;

    unsigned xoff[4], woff[3];
#pragma unroll
    for (int i = 0; i < 4; ++i) {
        const int row = wid * 32 + i * 8 + (lane >> 3);
        const int unit = (lane & 7) ^ ((row >> 1) & 7);
        xoff[i] = (unsigned)((rb * 128 + row) * (D_ * 2) + unit * 16);
    }
#pragma unroll
    for (int i = 0; i < 3; ++i) {
        const int u = (wid * 3 + i) * 64 + lane, row = u / 12, unit = u % 12;
        woff[i] = (unsigned)(row * (3 * D_ * 2) + (unit >> 2) * (D_ * 2) + hp * 64 + (unit & 3) * 16);
    }
    auto stage = [&](int c) {
        const unsigned slot = lds0 + (unsigned)((c % QK_NSLOT) * QK_SLOT);
        const char* xs = (const char*)x + c * 128;
        const char* wsrc = (const char*)w + (size_t)c * 64 * (3 * D_ * 2);
#pragma unroll
        for (int i = 0; i < 4; ++i) glds16(xs, xoff[i], slot + (unsigned)((wid * 4 + i) * 1024));
#pragma unroll
        for (int i = 0; i < 3; ++i) glds16(wsrc, woff[i], slot + 16384u + (unsigned)((wid * 3 + i) * 1024));
    };
#pragma unroll
    for (int c = 0; c < QK_NSLOT; ++c) stage(c);

    f32x16 accq = {}, acck = {}, accv = {};
    float s1 = 0.f, s2 = 0.f, csq = 0.f, csk = 0.f, csv = 0.f;
    const unsigned ONES = 0x3f803f80u;
    const int xrow = wid * 32 + r;
    const int xsw = (xrow >> 1) & 7;
    const int xbase = xrow * 128;
    const int wbase = 16384 + (8 * hh + ((lane & 15) >> 2)) * 192 + ((lane >> 4) & 1) * 32 + (lane & 3) * 8;

    for (int c = 0; c < 8; ++c) {
        if (c == 0) WAITV(28); else if (c <= 4) WAITV(21); else if (c == 5) WAITV(14); else if (c == 6) WAITV(7); else WAITV(0);
        BARRIER();
        if (c >= 1 && c + 4 < 8) stage(c + 4);
        lds_cptr slot = (lds_cptr)smem + (c % QK_NSLOT) * QK_SLOT;
#pragma unroll
        for (int ks = 0; ks < 4; ++ks) {
            const bf16x8 xf = lds_b128(slot + xbase + (((2 * ks + hh) ^ xsw) * 16));
            lds_cptr wp = slot + wbase + ks * 16 * 192;
            const bf16x8 wq = cat8(lds_tr(wp), lds_tr(wp + 4 * 192));
            const bf16x8 wk = cat8(lds_tr(wp + 64), lds_tr(wp + 64 + 4 * 192));
            const bf16x8 wv = cat8(lds_tr(wp + 128), lds_tr(wp + 128 + 4 * 192));
            accq = MFMA32(wq, xf, accq);
            acck = MFMA32(wk, xf, acck);
            accv = MFMA32(xf, wv, accv);
            const u32x4 xu = __builtin_bit_cast(u32x4, xf), qu = __builtin_bit_cast(u32x4, wq),
                        ku = __builtin_bit_cast(u32x4, wk), vu = __builtin_bit_cast(u32x4, wv);
#pragma unroll
            for (int j = 0; j < 4; ++j) {
                s1 = dot2bf(xu[j], ONES, s1); s2 = dot2bf(xu[j], xu[j], s2);
                csq = dot2bf(qu[j], ONES, csq); csk = dot2bf(ku[j], ONES, csk); csv = dot2bf(vu[j], ONES, csv);
            }
        }
    }
    s1 += other_half(s1); s2 += other_half(s2);
    csq += other_half(csq); csk += other_half(csk); csv += other_half(csv);
    const float mean = s1 * (1.f / D_);
    const float var = fmaxf(s2 * (1.f / D_) - mean * mean, 0.f);
    const float rstd = __builtin_amdgcn_rsqf(var + LN_EPS);
    LAS float* st = (LAS float*)(smem + QK_STAT) + wid * 160;
    if (hh == 0) { st[r] = csq; st[32 + r] = csk; st[64 + r] = mean; st[96 + r] = rstd; }
    asm volatile("s_waitcnt lgkmcnt(0)" ::: "memory");
    const int tile = rb * 4 + wid;
    {
        u32x4 oq[2], ok[2];
        const float rq = rstd * QSCALE;
#pragma unroll
        for (int g4 = 0; g4 < 4; ++g4) {
            const f32x4 cq = *(const LAS f32x4*)(st + 8 * g4 + 4 * hh);
            const f32x4 ck = *(const LAS f32x4*)(st + 32 + 8 * g4 + 4 * hh);
            float vq[4], vk[4];
#pragma unroll
            for (int i = 0; i < 4; ++i) {
                vq[i] = (accq[4 * g4 + i] - mean * cq[i]) * rq;
                vk[i] = (acck[4 * g4 + i] - mean * ck[i]) * rstd;
            }
            oq[g4 >> 1][(g4 & 1) * 2] = cvtpk(vq[0], vq[1]); oq[g4 >> 1][(g4 & 1) * 2 + 1] = cvtpk(vq[2], vq[3]);
            ok[g4 >> 1][(g4 & 1) * 2] = cvtpk(vk[0], vk[1]); ok[g4 >> 1][(g4 & 1) * 2 + 1] = cvtpk(vk[2], vk[3]);
        }
#pragma unroll
        for (int hd = 0; hd < 2; ++hd) {
            const size_t o = ((size_t)(2 * hp + hd) * 64 + tile) * 1024 + lane * 16;
            *(u32x4*)(ws + WS_Q + o) = oq[hd];
            *(u32x4*)(ws + WS_K + o) = ok[hd];
        }
    }
    {
        u32x4 ov[2];
#pragma unroll
        for (int g4 = 0; g4 < 4; ++g4) {
            const f32x4 mm = *(const LAS f32x4*)(st + 64 + 8 * g4 + 4 * hh);
            const f32x4 rr = *(const LAS f32x4*)(st + 96 + 8 * g4 + 4 * hh);
            float vv[4];
#pragma unroll
            for (int i = 0; i < 4; ++i) vv[i] = (accv[4 * g4 + i] - mm[i] * csv) * rr[i];
            ov[g4 >> 1][(g4 & 1) * 2] = cvtpk(vv[0], vv[1]); ov[g4 >> 1][(g4 & 1) * 2 + 1] = cvtpk(vv[2], vv[3]);
        }
        char* vb = ws + WS_V + (size_t)(2 * hp + (r >> 4)) * VHEAD + (size_t)tile * 2 * VBLK;
#pragma unroll
        for (int s = 0; s < 2; ++s) *(u32x4*)(vb + s * VBLK + hh * 256 + (r & 15) * 16) = ov[s];
        if ((r & 15) == 0) *(u32x4*)(vb + hh * VBLK + 512) = (u32x4){ONES, ONES, ONES, ONES};
    }
}

constexpr int AT_V = 65536, AT_LDS = AT_V + VHEAD;

__global__ void __launch_bounds__(512, 2)
k_attn(char* __restrict__ ws) {
    extern __shared__ __attribute__((aligned(16))) char smem[];
    const int tid = threadIdx.x, lane = tid & 63, wid = uni(tid >> 6), r = lane & 31, hh = lane >> 5;
    const int b = blockIdx.x, xcd = b & 7, ii = b >> 3;
    const int head = xcd * 4 + (ii & 3), qb = ii >> 2;
    const unsigned lds0 = (unsigned)(uintptr_t)smem;
    const char* kg = ws + WS_K + (size_t)head * 65536;
    const char* vg = ws + WS_V + (size_t)head * VHEAD;
    const unsigned lo = (unsigned)lane * 16;
    if (wid < 2) glds16(vg + wid * 1024, lo, lds0 + AT_V + wid * 1024);
#pragma unroll
    for (int i = 0; i < 8; ++i) {
        const int jk = 8 * i + wid, jv = jk + 2;
        glds16(kg + jk * 1024, lo, lds0 + jk * 1024);
        glds16(vg + jv * 1024, lo, lds0 + AT_V + jv * 1024);
    }
    const int mt = qb * 8 + wid;
    bf16x8 qf;
    asm volatile("global_load_dwordx4 %0, %1, %2\n\ts_waitcnt vmcnt(0)" : "=&v"(qf)
                 : "v"(lo), "s"(ws + WS_Q + ((size_t)head * 64 + mt) * 1024) : "memory");
    BARRIER();

    lds_cptr kp = (lds_cptr)smem + lane * 16;
    lds_cptr vp = (lds_cptr)smem + AT_V + (r < 16 ? hh * 256 + r * 16 : 512);

    f32x16 zero = {};
    float m;
    {
        const f32x16 s0 = MFMA32(lds_b128(kp), qf, zero);
        m = s0[0];
#pragma unroll
        for (int g = 1; g < 16; ++g) m = fmaxf(m, s0[g]);
        m = fmaxf(m, other_half(m));
    }
    f32x16 o;
    for (int attempt = 0; attempt < 2; ++attempt) {
        f32x16 negm;
#pragma unroll
        for (int g = 0; g < 16; ++g) negm[g] = -m;
        o = zero;
        f32x16 s = MFMA32(lds_b128(kp), qf, negm);
#pragma unroll 2
        for (int t = 0; t < 64; ++t) {
            const int tn = (t + 1) & 63;
            const f32x16 sn = MFMA32(lds_b128(kp + tn * 1024), qf, negm);
            const bf16x8 v0 = lds_b128(vp + t * 2 * VBLK), v1 = lds_b128(vp + (t * 2 + 1) * VBLK);
            u32x4 p0, p1;
#pragma unroll
            for (int j = 0; j < 4; ++j) {
                p0[j] = cvtpk(__builtin_amdgcn_exp2f(s[2 * j]), __builtin_amdgcn_exp2f(s[2 * j + 1]));
                p1[j] = cvtpk(__builtin_amdgcn_exp2f(s[8 + 2 * j]), __builtin_amdgcn_exp2f(s[8 + 2 * j + 1]));
            }
            o = MFMA32(v0, __builtin_bit_cast(bf16x8, p0), o);
            o = MFMA32(v1, __builtin_bit_cast(bf16x8, p1), o);
            s = sn;
        }
        float amax = 0.f;
#pragma unroll
        for (int g = 0; g < 9; ++g) amax = fmaxf(amax, fabsf(o[g]));
        bool ok = true;
#pragma unroll
        for (int g = 0; g < 9; ++g) ok = ok && (fabsf(o[g]) < 1e30f);
        if (attempt == 1 || __all(ok)) break;
        float mx = -INFINITY;
        for (int t = 0; t < 64; ++t) {
            const f32x16 sx = MFMA32(lds_b128(kp + t * 1024), qf, zero);
#pragma unroll
            for (int g = 0; g < 16; ++g) mx = fmaxf(mx, sx[g]);
        }
        m = fmaxf(mx, other_half(mx));
    }
    const float rl = 1.0f / o[8];
    u32x4 ob;
#pragma unroll
    for (int j = 0; j < 4; ++j) ob[j] = cvtpk(o[2 * j] * rl, o[2 * j + 1] * rl);
    *(u32x4*)(ws + WS_O + ((size_t)mt * 32 + head) * 1024 + lane * 16) = ob;
}

constexpr int G2_SLOT = 32768, G2_NSLOT = 4, G2_LDS = G2_SLOT * G2_NSLOT + 256;

template <int NCH, bool FINAL>
__device__ __forceinline__ void gemm_n512(const char* __restrict__ a, const u16* __restrict__ w, const u16* __restrict__ x,
                                          char* __restrict__ ws, u16* __restrict__ out, char* smem, int b,
                                          const u16* __restrict__ wg, const u16* __restrict__ wu) {
    const int tid = threadIdx.x, lane = tid & 63, wid = uni(tid >> 6), r = lane & 31, hh = lane >> 5;
    const int xcd = b & 7, ii = b >> 3;
    const int rb = (xcd >> 1) * 8 + (ii >> 2), cb = (xcd & 1) * 4 + (ii & 3);
    constexpr int KS = NCH * 8;
    const unsigned lds0 = (unsigned)(uintptr_t)smem;
    const unsigned aoff = (unsigned)(((2 * rb + (wid >> 1)) * KS + (wid & 1) * 4) * 1024 + lane * 16);
    unsigned woff[4];
#pragma unroll
    for (int i = 0; i < 4; ++i) {
        const int row = wid * 32 + i * 8 + (lane >> 3);
        const int unit = (lane & 7) ^ (((row >> 1) & 1) << 2);
        woff[i] = (unsigned)(row * (D_ * 2) + cb * 128 + unit * 16);
    }
    auto stage = [&](int c) {
        const unsigned slot = lds0 + (unsigned)((c % G2_NSLOT) * G2_SLOT);
        const char* as = a + (size_t)c * 8192;
        const char* wsrc = (const char*)w + (size_t)c * 128 * (D_ * 2);
#pragma unroll
        for (int i = 0; i < 4; ++i) glds16(as + i * 1024, aoff, slot + (unsigned)((wid * 4 + i) * 1024));
#pragma unroll
        for (int i = 0; i < 4; ++i) glds16(wsrc, woff[i], slot + 16384u + (unsigned)((wid * 4 + i) * 1024));
    };
    u32x4 cv[4];
    const int cmat = b >= 112 ? 1 : 0, cc0 = (b - cmat * 112) * 16;
    if (!FINAL) {
        const char* cw = (const char*)(cmat ? wu : wg);
        const unsigned co = (unsigned)((tid >> 1) * (F_ * 2) + (b < 224 ? cc0 : 0) * 2 + (tid & 1) * 16);
#pragma unroll
        for (int i = 0; i < 4; ++i)
            asm volatile("global_load_dwordx4 %0, %1, %2" : "=&v"(cv[i]) : "v"(co), "s"(cw + (size_t)i * 128 * (F_ * 2)) : "memory");
    }
#pragma unroll
    for (int c = 0; c < G2_NSLOT; ++c) stage(c);
    if (!FINAL) {
        asm volatile("s_waitcnt vmcnt(32)" : "+v"(cv[0]), "+v"(cv[1]), "+v"(cv[2]), "+v"(cv[3]) :: "memory");
        float cs[8];
#pragma unroll
        for (int j = 0; j < 4; ++j) {
            cs[2 * j] = (bf_lo(cv[0][j]) + bf_lo(cv[1][j])) + (bf_lo(cv[2][j]) + bf_lo(cv[3][j]));
            cs[2 * j + 1] = (bf_hi(cv[0][j]) + bf_hi(cv[1][j])) + (bf_hi(cv[2][j]) + bf_hi(cv[3][j]));
        }
#pragma unroll
        for (int d = 2; d < 64; d <<= 1)
#pragma unroll
            for (int j = 0; j < 8; ++j) cs[j] += __shfl_xor(cs[j], d);
        LAS float* red = (LAS float*)(smem + G2_SLOT * G2_NSLOT) + wid * 16 + (lane & 1) * 8;
        if (lane < 2) { *(LAS f32x4*)red = (f32x4){cs[0], cs[1], cs[2], cs[3]}; *(LAS f32x4*)(red + 4) = (f32x4){cs[4], cs[5], cs[6], cs[7]}; }
    }

    const int wm = wid >> 1, wn = wid & 1;
    f32x16 acc = {};
    const int abase = wm * 8192 + lane * 16;
    const int q = (lane & 15) >> 2;
    const int wbase = 16384 + (4 * hh + q) * 128 + ((wn ^ ((q >> 1) & 1)) * 64) + ((lane >> 4) & 1) * 32 + (lane & 3) * 8;

    for (int c = 0; c < NCH; ++c) {
        const int rem = NCH - 1 - c;
        if (c == 0) WAITV(24); else if (rem >= 2) WAITV(16); else if (rem == 1) WAITV(8); else WAITV(0);
        BARRIER();
        if (c >= 1 && c + 3 < NCH) stage(c + 3);
        lds_cptr slot = (lds_cptr)smem + (c % G2_NSLOT) * G2_SLOT;
#pragma unroll
        for (int ks = 0; ks < 8; ++ks) {
            const bf16x8 af = lds_b128(slot + abase + ks * 1024);
            lds_cptr wp = slot + wbase + ks * 16 * 128;
            const bf16x8 wf = cat8(lds_tr(wp), lds_tr(wp + 8 * 128));
            acc = MFMA32(wf, af, acc);
        }
    }
    const int mt = 2 * rb + wm, n0 = cb * 64 + wn * 32, tok = mt * 32 + r;
    if (!FINAL) {
        if (tid < 16 && b < 224) {
            const LAS float* red = (const LAS float*)(smem + G2_SLOT * G2_NSLOT) + tid;
            ((float*)(ws + WS_CS))[cmat * F_ + cc0 + tid] = (red[0] + red[16]) + (red[32] + red[48]);
        }
        u32x4 ob[2];
#pragma unroll
        for (int g4 = 0; g4 < 4; ++g4) {
            const u32x2 xv = *(const u32x2*)(x + (size_t)tok * D_ + n0 + 8 * g4 + 4 * hh);
            ob[g4 >> 1][(g4 & 1) * 2] = cvtpk(acc[4 * g4] + bf_lo(xv[0]), acc[4 * g4 + 1] + bf_hi(xv[0]));
            ob[g4 >> 1][(g4 & 1) * 2 + 1] = cvtpk(acc[4 * g4 + 2] + bf_lo(xv[1]), acc[4 * g4 + 3] + bf_hi(xv[1]));
        }
#pragma unroll
        for (int s = 0; s < 2; ++s) *(u32x4*)(ws + WS_H + ((size_t)mt * 32 + n0 / 16 + s) * 1024 + lane * 16) = ob[s];
    } else {
#pragma unroll
        for (int s = 0; s < 2; ++s) {
            const u32x4 hv = *(const u32x4*)(ws + WS_H + ((size_t)mt * 32 + n0 / 16 + s) * 1024 + lane * 16);
#pragma unroll
            for (int j = 0; j < 2; ++j) {
                const int g4 = 2 * s + j;
                u32x2 ov;
                ov[0] = cvtpk(acc[4 * g4] + bf_lo(hv[2 * j]), acc[4 * g4 + 1] + bf_hi(hv[2 * j]));
                ov[1] = cvtpk(acc[4 * g4 + 2] + bf_lo(hv[2 * j + 1]), acc[4 * g4 + 3] + bf_hi(hv[2 * j + 1]));
                *(u32x2*)(out + (size_t)tok * D_ + n0 + 8 * g4 + 4 * hh) = ov;
            }
        }
    }
}

__global__ void __launch_bounds__(256, 1)
k_wo(const u16* __restrict__ x, const u16* __restrict__ wo, const u16* __restrict__ wg, const u16* __restrict__ wu,
     char* __restrict__ ws) {
    extern __shared__ __attribute__((aligned(16))) char smem[];
    gemm_n512<4, false>(ws + WS_O, wo, x, ws, nullptr, smem, blockIdx.x, wg, wu);
}

__global__ void __launch_bounds__(256, 1)
k_ffn2(const u16* __restrict__ wd, char* __restrict__ ws, u16* __restrict__ out) {
    extern __shared__ __attribute__((aligned(16))) char smem[];
    gemm_n512<14, true>(ws + WS_A, wd, nullptr, ws, out, smem, blockIdx.x, nullptr, nullptr);
}

constexpr int F1_UP = 14336 + 128, F1_SLOT = F1_UP + 14336 + 128, F1_NSLOT = 5, F1_LDS = F1_SLOT * F1_NSLOT;

__device__ __forceinline__ float gelu_tanh(float v) {
    const float c1 = -2.0f * 0.7978845608028654f * 1.4426950408889634f;
    const float c3 = c1 * 0.044715f;
    const float e = __builtin_amdgcn_exp2f(v * (c1 + c3 * v * v));
    return v * __builtin_amdgcn_rcpf(1.0f + e);
}

__global__ void __launch_bounds__(256, 1)
k_ffn1(const u16* __restrict__ wg, const u16* __restrict__ wu, char* __restrict__ ws) {
    extern __shared__ __attribute__((aligned(16))) char smem[];
    const int tid = threadIdx.x, lane = tid & 63, wid = uni(tid >> 6), r = lane & 31, hh = lane >> 5;
    const int b = blockIdx.x, xcd = b & 7, ii = b >> 3;
    const int rb = (xcd >> 2) * 8 + (ii >> 2), cb = (xcd & 3) * 4 + (ii & 3);
    const unsigned lds0 = (unsigned)(uintptr_t)smem;
    const int mt = rb * 4 + wid;

    bf16x8 af[32];
    {
        const char* hsrc = ws + WS_H + (size_t)mt * 32 * 1024;
        const unsigned lo = (unsigned)lane * 16;
#pragma unroll
        for (int i = 0; i < 8; ++i) {
            const char* p = hsrc + i * 4096;
            asm volatile("global_load_dwordx4 %0, %4, %5\n\tglobal_load_dwordx4 %1, %4, %5 offset:1024\n\t"
                         "global_load_dwordx4 %2, %4, %5 offset:2048\n\tglobal_load_dwordx4 %3, %4, %5 offset:3072"
                         : "=&v"(af[4 * i]), "=&v"(af[4 * i + 1]), "=&v"(af[4 * i + 2]), "=&v"(af[4 * i + 3])
                         : "v"(lo), "s"(p) : "memory");
        }
    }
    const char* wsrc0 = (const char*)(wid < 2 ? wg : wu);
    unsigned woff[7];
#pragma unroll
    for (int i = 0; i < 7; ++i) {
        const int u = ((wid & 1) * 7 + i) * 64 + lane, row = u / 14, unit = u % 14;
        woff[i] = (unsigned)(row * (F_ * 2) + cb * 224 + unit * 16);
    }
    const unsigned wdst = (unsigned)((wid >> 1) * F1_UP + (wid & 1) * 7 * 1024);
    auto stage = [&](int c) {
        const unsigned slot = lds0 + (unsigned)((c % F1_NSLOT) * F1_SLOT) + wdst;
        const char* wsrc = wsrc0 + (size_t)c * 64 * (F_ * 2);
#pragma unroll
        for (int i = 0; i < 7; ++i) glds16(wsrc, woff[i], slot + (unsigned)(i * 1024));
    };
#pragma unroll
    for (int c = 0; c < 4; ++c) stage(c);

    f32x16 acc[7];
#pragma unroll
    for (int j = 0; j < 7; ++j) acc[j] = (f32x16){};
    float s1 = 0.f, s2 = 0.f;
    const unsigned ONES = 0x3f803f80u;
    const int wbase = ((lane >> 4) & 1) * F1_UP + (4 * hh + ((lane & 15) >> 2)) * 224 + (lane & 3) * 8;

#pragma unroll
    for (int c = 0; c < 8; ++c) {
        if (c <= 4) WAITV(21); else if (c == 5) WAITV(14); else if (c == 6) WAITV(7); else WAITV(0);
        BARRIER();
        if (c == 0) {
#pragma unroll
            for (int i = 0; i < 32; ++i) asm volatile("" : "+v"(af[i]));
        }
        if (c + 4 < 8) stage(c + 4);
        lds_cptr slot = (lds_cptr)smem + (c % F1_NSLOT) * F1_SLOT;
#pragma unroll
        for (int ks = 0; ks < 4; ++ks) {
            const bf16x8 xf = af[c * 4 + ks];
            lds_cptr wp = slot + wbase + ks * 16 * 224;
#pragma unroll
            for (int j = 0; j < 7; ++j) {
                const bf16x8 wf = cat8(lds_tr(wp + j * 32), lds_tr(wp + j * 32 + 8 * 224));
                acc[j] = MFMA32(wf, xf, acc[j]);
            }
            const u32x4 xu = __builtin_bit_cast(u32x4, xf);
#pragma unroll
            for (int j = 0; j < 4; ++j) { s1 = dot2bf(xu[j], ONES, s1); s2 = dot2bf(xu[j], xu[j], s2); }
        }
    }
    s1 += other_half(s1); s2 += other_half(s2);
    const float mean = s1 * (1.f / D_);
    const float var = fmaxf(s2 * (1.f / D_) - mean * mean, 0.f);
    const float rstd = __builtin_amdgcn_rsqf(var + LN_EPS);
    const float* cs = (const float*)(ws + WS_CS);
#pragma unroll
    for (int j = 0; j < 7; ++j) {
        const int f0 = cb * 112 + j * 16;
        u32x4 ob;
#pragma unroll
        for (int g4 = 0; g4 < 2; ++g4) {
            const f32x4 cg = *(const f32x4*)(cs + f0 + 8 * g4 + 4 * hh);
            const f32x4 cu = *(const f32x4*)(cs + F_ + f0 + 8 * g4 + 4 * hh);
            float a[4];
#pragma unroll
            for (int i = 0; i < 4; ++i) {
                const float gv = (acc[j][4 * g4 + i] - mean * cg[i]) * rstd;
                const float uv = (acc[j][8 + 4 * g4 + i] - mean * cu[i]) * rstd;
                a[i] = gelu_tanh(gv) * uv;
            }
            ob[2 * g4] = cvtpk(a[0], a[1]); ob[2 * g4 + 1] = cvtpk(a[2], a[3]);
        }
        *(u32x4*)(ws + WS_A + ((size_t)mt * 112 + cb * 7 + j) * 1024 + lane * 16) = ob;
    }
}

extern "C" void kernel_launch(void* const* d_in, const int* in_sizes, int n_in,
                              void* d_out, int out_size, void* d_ws, size_t ws_size,
                              hipStream_t stream) {
    const u16* x = (const u16*)d_in[0];
    const u16* wqkv = (const u16*)d_in[1];
    const u16* wo = (const u16*)d_in[2];
    const u16* wg = (const u16*)d_in[4];
    const u16* wu = (const u16*)d_in[5];
    const u16* wd = (const u16*)d_in[6];
    char* ws = (char*)d_ws;
    (void)hipFuncSetAttribute((const void*)k_qkv, hipFuncAttributeMaxDynamicSharedMemorySize, QK_LDS);
    (void)hipFuncSetAttribute((const void*)k_attn, hipFuncAttributeMaxDynamicSharedMemorySize, AT_LDS);
    (void)hipFuncSetAttribute((const void*)k_wo, hipFuncAttributeMaxDynamicSharedMemorySize, G2_LDS);
    (void)hipFuncSetAttribute((const void*)k_ffn1, hipFuncAttributeMaxDynamicSharedMemorySize, F1_LDS);
    (void)hipFuncSetAttribute((const void*)k_ffn2, hipFuncAttributeMaxDynamicSharedMemorySize, G2_LDS);
    k_qkv<<<256, 256, QK_LDS, stream>>>(x, wqkv, ws);
    k_attn<<<256, 512, AT_LDS, stream>>>(ws);
    k_wo<<<256, 256, G2_LDS, stream>>>(x, wo, wg, wu, ws);
    k_ffn1<<<256, 256, F1_LDS, stream>>>(wg, wu, ws);
    k_ffn2<<<256, 256, G2_LDS, stream>>>(wd, ws, (u16*)d_out);
}
```

```cpp
#include <hip/hip_runtime.h>
#include <hip/hip_bf16.h>
#include <stdint.h>

typedef __attribute__((ext_vector_type(8))) short bf16x8;
typedef __attribute__((ext_vector_type(4))) short s16x4;
typedef __attribute__((ext_vector_type(16))) float f32x16;
typedef __attribute__((ext_vector_type(4))) float f32x4;
typedef __attribute__((ext_vector_type(2))) float f32x2;
typedef __attribute__((ext_vector_type(2))) __bf16 bf16x2;
typedef __attribute__((ext_vector_type(8))) __bf16 bf16v8;
typedef __attribute__((ext_vector_type(16))) __bf16 bf16v16;
typedef __attribute__((ext_vector_type(8))) float f32x8;
typedef __attribute__((ext_vector_type(4))) unsigned u32x4;
typedef __attribute__((ext_vector_type(2))) unsigned u32x2;
typedef __attribute__((ext_vector_type(16))) unsigned u32x16;
typedef unsigned short u16;
typedef unsigned long long u64;
#define LAS __attribute__((address_space(3)))
#define GAS __attribute__((address_space(1)))
typedef LAS const char* lds_cptr;
typedef LAS char* lds_ptr;

constexpr int D_ = 512, F_ = 1792;
constexpr float LN_EPS = 1e-5f;
constexpr float QSCALE = 0.25f * 1.4426950408889634f;

constexpr size_t WS_EPOCH = 0;
constexpr size_t WS_FKV = 4096;
constexpr size_t WS_FO = WS_FKV + 32768;
constexpr size_t WS_FH = WS_FO + 32768;
constexpr size_t WS_FA = WS_FH + 32768;
constexpr size_t WS_XS = WS_FA + 32768;
constexpr size_t WS_HS = WS_XS + 32768;
constexpr size_t WS_KV = WS_HS + 262144;
constexpr size_t WS_O = WS_KV + (size_t)32 * 64 * 2048;
constexpr size_t WS_H = WS_O + (size_t)64 * 32 * 1024;
constexpr size_t WS_A = WS_H + (size_t)64 * 32 * 1024;

constexpr int LDS_BYTES = 163840;

#define TOUCH0 0
#define ABL 0
#define YDELAY 0
#define P5AW 6
#define HMAP 0
#define STAMP_DECL
#define STAMP(i)
#define STAMP_OUT()

#define MFMA32(a, b, c) __builtin_amdgcn_mfma_f32_32x32x16_bf16(a, b, c, 0, 0, 0)
#define MFMA16(a, b, c) __builtin_amdgcn_mfma_f32_16x16x32_bf16(a, b, c, 0, 0, 0)
#define WAITV(n) asm volatile("s_waitcnt vmcnt(%0)" :: "n"(n) : "memory")
#define BARRIER() asm volatile("s_waitcnt lgkmcnt(0)\n\ts_barrier" ::: "memory")
#define PIN(x) asm volatile("" : "+v"(x))
#define SBAR() __builtin_amdgcn_sched_barrier(0)

__device__ __forceinline__ void glds16(const void* sbase, unsigned voff, unsigned lds_base) {
    unsigned sv;
    asm volatile("s_mov_b32 %0, m0\n\ts_mov_b32 m0, %3\n\ts_nop 0\n\tglobal_load_lds_dwordx4 %1, %2\n\ts_mov_b32 m0, %0"
                 : "=&s"(sv) : "v"(voff), "s"(sbase), "s"(lds_base) : "memory");
}
__device__ __forceinline__ void glds16_sc1(const void* sbase, unsigned voff, unsigned lds_base) {
    unsigned sv;
    asm volatile("s_mov_b32 %0, m0\n\ts_mov_b32 m0, %3\n\ts_nop 0\n\tglobal_load_lds_dwordx4 %1, %2 sc1\n\ts_mov_b32 m0, %0"
                 : "=&s"(sv) : "v"(voff), "s"(sbase), "s"(lds_base) : "memory");
}
__device__ __forceinline__ void glds16x4_sc1(const void* sbase, unsigned voff, unsigned lds_base) {
    unsigned sv;
    asm volatile("s_mov_b32 %0, m0\n\ts_mov_b32 m0, %3\n\ts_nop 0\n\tglobal_load_lds_dwordx4 %1, %2 sc1\n\t"
                 "global_load_lds_dwordx4 %1, %2 offset:1024 sc1\n\tglobal_load_lds_dwordx4 %1, %2 offset:2048 sc1\n\t"
                 "global_load_lds_dwordx4 %1, %2 offset:3072 sc1\n\ts_mov_b32 m0, %0"
                 : "=&s"(sv) : "v"(voff), "s"(sbase), "s"(lds_base) : "memory");
}
__device__ __forceinline__ void touch(const void* sbase, unsigned voff, unsigned lds_junk) {
    unsigned sv;
    asm volatile("s_mov_b32 %0, m0\n\ts_mov_b32 m0, %3\n\ts_nop 0\n\tglobal_load_lds_dword %1, %2\n\ts_mov_b32 m0, %0"
                 : "=&s"(sv) : "v"(voff), "s"(sbase), "s"(lds_junk) : "memory");
}
__device__ __forceinline__ void touch_sc1(const void* sbase, unsigned voff, unsigned lds_junk) {
    unsigned sv;
    asm volatile("s_mov_b32 %0, m0\n\ts_mov_b32 m0, %3\n\ts_nop 0\n\tglobal_load_lds_dword %1, %2 sc1\n\ts_mov_b32 m0, %0"
                 : "=&s"(sv) : "v"(voff), "s"(sbase), "s"(lds_junk) : "memory");
}
template <typename T> __device__ __forceinline__ void gload16_sc1(T& d, const void* sbase, unsigned voff) {
    asm volatile("global_load_dwordx4 %0, %1, %2 sc1" : "=&v"(d) : "v"(voff), "s"(sbase) : "memory");
}
template <typename T> __device__ __forceinline__ void gload16(T& d, const void* sbase, unsigned voff) {
    asm volatile("global_load_dwordx4 %0, %1, %2" : "=&v"(d) : "v"(voff), "s"(sbase) : "memory");
}
__device__ __forceinline__ void gload8(u32x2& d, const void* sbase, unsigned voff) {
    asm volatile("global_load_dwordx2 %0, %1, %2" : "=&v"(d) : "v"(voff), "s"(sbase) : "memory");
}
__device__ __forceinline__ void gstore16_sc1(void* sbase, unsigned voff, u32x4 v) {
    asm volatile("global_store_dwordx4 %0, %1, %2 sc1\n\ts_nop 1" :: "v"(voff), "v"(v), "s"(sbase) : "memory");
}
__device__ __forceinline__ void gstore8_sc1(void* sbase, unsigned voff, u32x2 v) {
    asm volatile("global_store_dwordx2 %0, %1, %2 sc1\n\ts_nop 1" :: "v"(voff), "v"(v), "s"(sbase) : "memory");
}
__device__ __forceinline__ unsigned cvtpk(float lo, float hi) {
    return __builtin_bit_cast(unsigned, __builtin_convertvector((f32x2){lo, hi}, bf16x2));
}
__device__ __forceinline__ bf16x8 lds_b128(lds_cptr p) { return *(const LAS bf16x8*)p; }
__device__ __forceinline__ s16x4 lds_tr(lds_cptr p) { return __builtin_amdgcn_ds_read_tr16_b64_v4i16((LAS s16x4*)p); }
__device__ __forceinline__ bf16x8 cat8(s16x4 a, s16x4 b) { return (bf16x8){a[0], a[1], a[2], a[3], b[0], b[1], b[2], b[3]}; }
__device__ __forceinline__ float dot2bf(unsigned a, unsigned b, float c) {
    return __builtin_amdgcn_fdot2_f32_bf16(__builtin_bit_cast(bf16x2, a), __builtin_bit_cast(bf16x2, b), c, false);
}
__device__ __forceinline__ float sum_halves(float v) {
    auto p = __builtin_amdgcn_permlane32_swap(__float_as_uint(v), __float_as_uint(v), false, false);
    return __uint_as_float(p[0]) + __uint_as_float(p[1]);
}
__device__ __forceinline__ float max_halves(float v) {
    auto p = __builtin_amdgcn_permlane32_swap(__float_as_uint(v), __float_as_uint(v), false, false);
    return fmaxf(__uint_as_float(p[0]), __uint_as_float(p[1]));
}
template <int CTRL> __device__ __forceinline__ float dpp_f(float v) {
    return __int_as_float(__builtin_amdgcn_update_dpp(0, __float_as_int(v), CTRL, 0xf, 0xf, true));
}
__device__ __forceinline__ float wave_sum(float v) {
    v += dpp_f<0xB1>(v); v += dpp_f<0x4E>(v); v += dpp_f<0x141>(v); v += dpp_f<0x140>(v);
    const int i = __float_as_int(v);
    return (__int_as_float(__builtin_amdgcn_readlane(i, 0)) + __int_as_float(__builtin_amdgcn_readlane(i, 16))) +
           (__int_as_float(__builtin_amdgcn_readlane(i, 32)) + __int_as_float(__builtin_amdgcn_readlane(i, 48)));
}
__device__ __forceinline__ float sel(bool c, float a, float b) { PIN(a); PIN(b); return c ? a : b; }
__device__ __forceinline__ float bf_lo(unsigned u) { return __uint_as_float(u << 16); }
__device__ __forceinline__ float bf_hi(unsigned u) { return __uint_as_float(u & 0xffff0000u); }
__device__ __forceinline__ int uni(int v) { return __builtin_amdgcn_readfirstlane(v); }

template <int N> struct IntC { static constexpr int value = N; };
constexpr int SPIN_CAP = 1 << 17;
__device__ __forceinline__ void wait_flags(const char* base, unsigned off, bool active, u64 tag) {
    for (int spins = 0; spins < SPIN_CAP; ++spins) {
        const u64 v = __hip_atomic_load((const GAS u64*)(base + off), __ATOMIC_RELAXED, __HIP_MEMORY_SCOPE_AGENT);
        if (__all(!active || v == tag)) break;
        __builtin_amdgcn_s_sleep(1);
    }
}
__device__ __forceinline__ void set_flag(char* p, u64 tag) {
    __hip_atomic_store((GAS u64*)p, tag, __ATOMIC_RELAXED, __HIP_MEMORY_SCOPE_AGENT);
}

constexpr float GELU_C1 = -2.0f * 0.7978845608028654f * 1.4426950408889634f, GELU_C3 = GELU_C1 * 0.044715f;
__device__ __forceinline__ float fmul1(float a, float b) { float r = a * b; PIN(r); return r; }
__device__ __forceinline__ float ffma1(float a, float b, float c) { float r = __builtin_fmaf(a, b, c); PIN(r); return r; }

#define ATTN_LOOP_ASM \
    "s_nop 4\n\t" \
    "s_mov_b32 s48, m0\n\t" \
    "s_mov_b32 m0, %[pl]\n\t" \
    "global_load_dwordx4 v[156:159], v153, s[40:41] offset:-4096 sc1\n\t" \
    "s_mov_b64 exec, %[em]\n\t" \
    "global_load_dwordx4 v[172:175], v154, s[40:41] offset:-4096 sc1\n\t" \
    "global_load_dwordx4 v[176:179], v154, s[40:41] offset:-3584 sc1\n\t" \
    "s_mov_b64 exec, -1\n\t" \
    "global_load_dwordx4 v[160:163], v153, s[40:41] offset:-2048 sc1\n\t" \
    "global_load_lds_dwordx4 v155, s[46:47]\n\t" \
    "s_add_u32 m0, m0, 0x400\n\t" \
    "s_add_u32 s46, s46, %[ps]\n\t" \
    "s_addc_u32 s47, s47, 0\n\t" \
    "s_mov_b64 exec, %[em]\n\t" \
    "global_load_dwordx4 v[180:183], v154, s[40:41] offset:-2048 sc1\n\t" \
    "global_load_dwordx4 v[184:187], v154, s[40:41] offset:-1536 sc1\n\t" \
    "s_mov_b64 exec, -1\n\t" \
    "global_load_dwordx4 v[164:167], v153, s[40:41] offset:0 sc1\n\t" \
    "s_mov_b64 exec, %[em]\n\t" \
    "global_load_dwordx4 v[188:191], v154, s[40:41] offset:0 sc1\n\t" \
    "global_load_dwordx4 v[192:195], v154, s[40:41] offset:512 sc1\n\t" \
    "s_mov_b64 exec, -1\n\t" \
    "global_load_dwordx4 v[168:171], v153, s[40:41] offset:2048 sc1\n\t" \
    "global_load_lds_dwordx4 v155, s[46:47]\n\t" \
    "s_add_u32 m0, m0, 0x400\n\t" \
    "s_add_u32 s46, s46, %[ps]\n\t" \
    "s_addc_u32 s47, s47, 0\n\t" \
    "s_waitcnt vmcnt(11)\n\t" \
    "v_mfma_f32_32x32x16_bf16 v[80:95], v[156:159], v[144:147], v[48:63]\n\t" \
    "v_mfma_f32_32x32x16_bf16 v[96:111], v[156:159], v[148:151], v[64:79]\n\t" \
    "v_mov_b32 v136, 0\n\t" \
    "v_mov_b32 v137, 0\n\t" \
    "v_mov_b32 v138, 0\n\t" \
    "v_mov_b32 v139, 0\n\t" \
    "v_mov_b32 v140, 0\n\t" \
    "v_mov_b32 v141, 0\n\t" \
    "v_mov_b32 v142, 0\n\t" \
    "v_mov_b32 v143, 0\n\t" \
    "s_nop 3\n\t" \
    "1:\n\t" \
    "v_exp_f32 v80, v80\n\t" \
    "v_exp_f32 v81, v81\n\t" \
    "v_exp_f32 v82, v82\n\t" \
    "v_exp_f32 v83, v83\n\t" \
    "v_smfmac_f32_32x32x32_bf16 v[32:47], v[200:203], v[136:143], v152\n\t" \
    "s_waitcnt vmcnt(8)\n\t" \
    "s_mov_b64 exec, %[em]\n\t" \
    "global_load_dwordx4 v[196:199], v154, s[40:41] offset:2048 sc1\n\t" \
    "global_load_dwordx4 v[200:203], v154, s[40:41] offset:2560 sc1\n\t" \
    "s_mov_b64 exec, -1\n\t" \
    "v_exp_f32 v84, v84\n\t" \
    "v_exp_f32 v85, v85\n\t" \
    "v_exp_f32 v86, v86\n\t" \
    "v_exp_f32 v87, v87\n\t" \
    "v_exp_f32 v88, v88\n\t" \
    "v_exp_f32 v89, v89\n\t" \
    "v_smfmac_f32_32x32x32_bf16 v[16:31], v[172:175], v[80:87], v152\n\t" \
    "global_load_dwordx4 v[156:159], v153, s[42:43] offset:-4096 sc1\n\t" \
    "v_exp_f32 v90, v90\n\t" \
    "v_exp_f32 v91, v91\n\t" \
    "v_exp_f32 v92, v92\n\t" \
    "v_exp_f32 v93, v93\n\t" \
    "v_mfma_f32_32x32x16_bf16 v[112:127], v[160:163], v[144:147], v[48:63]\n\t" \
    "v_exp_f32 v94, v94\n\t" \
    "v_exp_f32 v95, v95\n\t" \
    "v_exp_f32 v96, v96\n\t" \
    "v_exp_f32 v97, v97\n\t" \
    "v_exp_f32 v98, v98\n\t" \
    "v_exp_f32 v99, v99\n\t" \
    "v_smfmac_f32_32x32x32_bf16 v[16:31], v[176:179], v[88:95], v152\n\t" \
    "v_exp_f32 v100, v100\n\t" \
    "v_exp_f32 v101, v101\n\t" \
    "v_exp_f32 v102, v102\n\t" \
    "v_exp_f32 v103, v103\n\t" \
    "v_exp_f32 v104, v104\n\t" \
    "v_exp_f32 v105, v105\n\t" \
    "v_smfmac_f32_32x32x32_bf16 v[32:47], v[172:175], v[96:103], v152\n\t" \
    "v_exp_f32 v106, v106\n\t" \
    "v_exp_f32 v107, v107\n\t" \
    "v_exp_f32 v108, v108\n\t" \
    "v_exp_f32 v109, v109\n\t" \
    "v_mfma_f32_32x32x16_bf16 v[128:143], v[160:163], v[148:151], v[64:79]\n\t" \
    "v_exp_f32 v110, v110\n\t" \
    "v_exp_f32 v111, v111\n\t" \
    "v_exp_f32 v112, v112\n\t" \
    "v_exp_f32 v113, v113\n\t" \
    "v_exp_f32 v114, v114\n\t" \
    "v_exp_f32 v115, v115\n\t" \
    "v_smfmac_f32_32x32x32_bf16 v[32:47], v[176:179], v[104:111], v152\n\t" \
    "s_waitcnt vmcnt(7)\n\t" \
    "s_mov_b64 exec, %[em]\n\t" \
    "global_load_dwordx4 v[172:175], v154, s[42:43] offset:-4096 sc1\n\t" \
    "global_load_dwordx4 v[176:179], v154, s[42:43] offset:-3584 sc1\n\t" \
    "s_mov_b64 exec, -1\n\t" \
    "v_exp_f32 v116, v116\n\t" \
    "v_exp_f32 v117, v117\n\t" \
    "v_exp_f32 v118, v118\n\t" \
    "v_exp_f32 v119, v119\n\t" \
    "v_exp_f32 v120, v120\n\t" \
    "v_exp_f32 v121, v121\n\t" \
    "v_smfmac_f32_32x32x32_bf16 v[16:31], v[180:183], v[112:119], v152\n\t" \
    "global_load_dwordx4 v[160:163], v153, s[42:43] offset:-2048 sc1\n\t" \
    "v_exp_f32 v122, v122\n\t" \
    "v_exp_f32 v123, v123\n\t" \
    "v_exp_f32 v124, v124\n\t" \
    "v_exp_f32 v125, v125\n\t" \
    "v_mfma_f32_32x32x16_bf16 v[80:95], v[164:167], v[144:147], v[48:63]\n\t" \
    "v_exp_f32 v126, v126\n\t" \
    "v_exp_f32 v127, v127\n\t" \
    "v_exp_f32 v128, v128\n\t" \
    "v_exp_f32 v129, v129\n\t" \
    "v_exp_f32 v130, v130\n\t" \
    "v_exp_f32 v131, v131\n\t" \
    "v_smfmac_f32_32x32x32_bf16 v[16:31], v[184:187], v[120:127], v152\n\t" \
    "v_exp_f32 v132, v132\n\t" \
    "v_exp_f32 v133, v133\n\t" \
    "v_exp_f32 v134, v134\n\t" \
    "v_exp_f32 v135, v135\n\t" \
    "v_exp_f32 v136, v136\n\t" \
    "v_exp_f32 v137, v137\n\t" \
    "v_smfmac_f32_32x32x32_bf16 v[32:47], v[180:183], v[128:135], v152\n\t" \
    "global_load_lds_dwordx4 v155, s[46:47]\n\t" \
    "s_add_u32 m0, m0, 0x400\n\t" \
    "s_add_u32 s46, s46, %[ps]\n\t" \
    "s_addc_u32 s47, s47, 0\n\t" \
    "v_exp_f32 v138, v138\n\t" \
    "v_exp_f32 v139, v139\n\t" \
    "v_exp_f32 v140, v140\n\t" \
    "v_exp_f32 v141, v141\n\t" \
    "v_mfma_f32_32x32x16_bf16 v[96:111], v[164:167], v[148:151], v[64:79]\n\t" \
    "v_exp_f32 v142, v142\n\t" \
    "v_exp_f32 v143, v143\n\t" \
    "v_exp_f32 v80, v80\n\t" \
    "v_exp_f32 v81, v81\n\t" \
    "v_exp_f32 v82, v82\n\t" \
    "v_exp_f32 v83, v83\n\t" \
    "v_smfmac_f32_32x32x32_bf16 v[32:47], v[184:187], v[136:143], v152\n\t" \
    "s_waitcnt vmcnt(8)\n\t" \
    "s_mov_b64 exec, %[em]\n\t" \
    "global_load_dwordx4 v[180:183], v154, s[42:43] offset:-2048 sc1\n\t" \
    "global_load_dwordx4 v[184:187], v154, s[42:43] offset:-1536 sc1\n\t" \
    "s_mov_b64 exec, -1\n\t" \
    "v_exp_f32 v84, v84\n\t" \
    "v_exp_f32 v85, v85\n\t" \
    "v_exp_f32 v86, v86\n\t" \
    "v_exp_f32 v87, v87\n\t" \
    "v_exp_f32 v88, v88\n\t" \
    "v_exp_f32 v89, v89\n\t" \
    "v_smfmac_f32_32x32x32_bf16 v[16:31], v[188:191], v[80:87], v152\n\t" \
    "global_load_dwordx4 v[164:167], v153, s[42:43] offset:0 sc1\n\t" \
    "v_exp_f32 v90, v90\n\t" \
    "v_exp_f32 v91, v91\n\t" \
    "v_exp_f32 v92, v92\n\t" \
    "v_exp_f32 v93, v93\n\t" \
    "v_mfma_f32_32x32x16_bf16 v[112:127], v[168:171], v[144:147], v[48:63]\n\t" \
    "v_exp_f32 v94, v94\n\t" \
    "v_exp_f32 v95, v95\n\t" \
    "v_exp_f32 v96, v96\n\t" \
    "v_exp_f32 v97, v97\n\t" \
    "v_exp_f32 v98, v98\n\t" \
    "v_exp_f32 v99, v99\n\t" \
    "v_smfmac_f32_32x32x32_bf16 v[16:31], v[192:195], v[88:95], v152\n\t" \
    "v_exp_f32 v100, v100\n\t" \
    "v_exp_f32 v101, v101\n\t" \
    "v_exp_f32 v102, v102\n\t" \
    "v_exp_f32 v103, v103\n\t" \
    "v_exp_f32 v104, v104\n\t" \
    "v_exp_f32 v105, v105\n\t" \
    "v_smfmac_f32_32x32x32_bf16 v[32:47], v[188:191], v[96:103], v152\n\t" \
    "v_exp_f32 v106, v106\n\t" \
    "v_exp_f32 v107, v107\n\t" \
    "v_exp_f32 v108, v108\n\t" \
    "v_exp_f32 v109, v109\n\t" \
    "v_mfma_f32_32x32x16_bf16 v[128:143], v[168:171], v[148:151], v[64:79]\n\t" \
    "v_exp_f32 v110, v110\n\t" \
    "v_exp_f32 v111, v111\n\t" \
    "v_exp_f32 v112, v112\n\t" \
    "v_exp_f32 v113, v113\n\t" \
    "v_exp_f32 v114, v114\n\t" \
    "v_exp_f32 v115, v115\n\t" \
    "v_smfmac_f32_32x32x32_bf16 v[32:47], v[192:195], v[104:111], v152\n\t" \
    "s_waitcnt vmcnt(7)\n\t" \
    "s_mov_b64 exec, %[em]\n\t" \
    "global_load_dwordx4 v[188:191], v154, s[42:43] offset:0 sc1\n\t" \
    "global_load_dwordx4 v[192:195], v154, s[42:43] offset:512 sc1\n\t" \
    "s_mov_b64 exec, -1\n\t" \
    "v_exp_f32 v116, v116\n\t" \
    "v_exp_f32 v117, v117\n\t" \
    "v_exp_f32 v118, v118\n\t" \
    "v_exp_f32 v119, v119\n\t" \
    "v_exp_f32 v120, v120\n\t" \
    "v_exp_f32 v121, v121\n\t" \
    "v_smfmac_f32_32x32x32_bf16 v[16:31], v[196:199], v[112:119], v152\n\t" \
    "global_load_dwordx4 v[168:171], v153, s[42:43] offset:2048 sc1\n\t" \
    "v_exp_f32 v122, v122\n\t" \
    "v_exp_f32 v123, v123\n\t" \
    "v_exp_f32 v124, v124\n\t" \
    "v_exp_f32 v125, v125\n\t" \
    "v_mfma_f32_32x32x16_bf16 v[80:95], v[156:159], v[144:147], v[48:63]\n\t" \
    "v_exp_f32 v126, v126\n\t" \
    "v_exp_f32 v127, v127\n\t" \
    "v_exp_f32 v128, v128\n\t" \
    "v_exp_f32 v129, v129\n\t" \
    "v_exp_f32 v130, v130\n\t" \
    "v_exp_f32 v131, v131\n\t" \
    "v_smfmac_f32_32x32x32_bf16 v[16:31], v[200:203], v[120:127], v152\n\t" \
    "v_exp_f32 v132, v132\n\t" \
    "v_exp_f32 v133, v133\n\t" \
    "v_exp_f32 v134, v134\n\t" \
    "v_exp_f32 v135, v135\n\t" \
    "v_exp_f32 v136, v136\n\t" \
    "v_exp_f32 v137, v137\n\t" \
    "v_smfmac_f32_32x32x32_bf16 v[32:47], v[196:199], v[128:135], v152\n\t" \
    "global_load_lds_dwordx4 v155, s[46:47]\n\t" \
    "s_add_u32 m0, m0, 0x400\n\t" \
    "s_add_u32 s46, s46, %[ps]\n\t" \
    "s_addc_u32 s47, s47, 0\n\t" \
    "s_mov_b64 s[40:41], s[42:43]\n\t" \
    "s_add_u32 %[g], %[g], 0x2000\n\t" \
    "s_and_b32 %[g], %[g], 0xffff\n\t" \
    "s_add_u32 s42, s44, %[g]\n\t" \
    "s_addc_u32 s43, s45, 0\n\t" \
    "s_sub_u32 %[n], %[n], 1\n\t" \
    "s_cmp_lg_u32 %[n], 0\n\t" \
    "v_exp_f32 v138, v138\n\t" \
    "v_exp_f32 v139, v139\n\t" \
    "v_exp_f32 v140, v140\n\t" \
    "v_exp_f32 v141, v141\n\t" \
    "v_mfma_f32_32x32x16_bf16 v[96:111], v[156:159], v[148:151], v[64:79]\n\t" \
    "v_exp_f32 v142, v142\n\t" \
    "v_exp_f32 v143, v143\n\t" \
    "s_cbranch_scc1 1b\n\t" \
    "v_exp_f32 v80, v80\n\t" \
    "v_exp_f32 v81, v81\n\t" \
    "v_exp_f32 v82, v82\n\t" \
    "v_exp_f32 v83, v83\n\t" \
    "v_smfmac_f32_32x32x32_bf16 v[32:47], v[200:203], v[136:143], v152\n\t" \
    "s_waitcnt vmcnt(8)\n\t" \
    "s_mov_b64 exec, %[em]\n\t" \
    "global_load_dwordx4 v[196:199], v154, s[40:41] offset:2048 sc1\n\t" \
    "global_load_dwordx4 v[200:203], v154, s[40:41] offset:2560 sc1\n\t" \
    "s_mov_b64 exec, -1\n\t" \
    "v_exp_f32 v84, v84\n\t" \
    "v_exp_f32 v85, v85\n\t" \
    "v_exp_f32 v86, v86\n\t" \
    "v_exp_f32 v87, v87\n\t" \
    "v_exp_f32 v88, v88\n\t" \
    "v_exp_f32 v89, v89\n\t" \
    "v_smfmac_f32_32x32x32_bf16 v[16:31], v[172:175], v[80:87], v152\n\t" \
    "v_exp_f32 v90, v90\n\t" \
    "v_exp_f32 v91, v91\n\t" \
    "v_exp_f32 v92, v92\n\t" \
    "v_exp_f32 v93, v93\n\t" \
    "v_mfma_f32_32x32x16_bf16 v[112:127], v[160:163], v[144:147], v[48:63]\n\t" \
    "v_exp_f32 v94, v94\n\t" \
    "v_exp_f32 v95, v95\n\t" \
    "v_exp_f32 v96, v96\n\t" \
    "v_exp_f32 v97, v97\n\t" \
    "v_exp_f32 v98, v98\n\t" \
    "v_exp_f32 v99, v99\n\t" \
    "v_smfmac_f32_32x32x32_bf16 v[16:31], v[176:179], v[88:95], v152\n\t" \
    "v_exp_f32 v100, v100\n\t" \
    "v_exp_f32 v101, v101\n\t" \
    "v_exp_f32 v102, v102\n\t" \
    "v_exp_f32 v103, v103\n\t" \
    "v_exp_f32 v104, v104\n\t" \
    "v_exp_f32 v105, v105\n\t" \
    "v_smfmac_f32_32x32x32_bf16 v[32:47], v[172:175], v[96:103], v152\n\t" \
    "v_exp_f32 v106, v106\n\t" \
    "v_exp_f32 v107, v107\n\t" \
    "v_exp_f32 v108, v108\n\t" \
    "v_exp_f32 v109, v109\n\t" \
    "v_mfma_f32_32x32x16_bf16 v[128:143], v[160:163], v[148:151], v[64:79]\n\t" \
    "v_exp_f32 v110, v110\n\t" \
    "v_exp_f32 v111, v111\n\t" \
    "v_exp_f32 v112, v112\n\t" \
    "v_exp_f32 v113, v113\n\t" \
    "v_exp_f32 v114, v114\n\t" \
    "v_exp_f32 v115, v115\n\t" \
    "v_smfmac_f32_32x32x32_bf16 v[32:47], v[176:179], v[104:111], v152\n\t" \
    "s_waitcnt vmcnt(6)\n\t" \
    "v_exp_f32 v116, v116\n\t" \
    "v_exp_f32 v117, v117\n\t" \
    "v_exp_f32 v118, v118\n\t" \
    "v_exp_f32 v119, v119\n\t" \
    "v_exp_f32 v120, v120\n\t" \
    "v_exp_f32 v121, v121\n\t" \
    "v_smfmac_f32_32x32x32_bf16 v[16:31], v[180:183], v[112:119], v152\n\t" \
    "v_exp_f32 v122, v122\n\t" \
    "v_exp_f32 v123, v123\n\t" \
    "v_exp_f32 v124, v124\n\t" \
    "v_exp_f32 v125, v125\n\t" \
    "v_mfma_f32_32x32x16_bf16 v[80:95], v[164:167], v[144:147], v[48:63]\n\t" \
    "v_exp_f32 v126, v126\n\t" \
    "v_exp_f32 v127, v127\n\t" \
    "v_exp_f32 v128, v128\n\t" \
    "v_exp_f32 v129, v129\n\t" \
    "v_exp_f32 v130, v130\n\t" \
    "v_exp_f32 v131, v131\n\t" \
    "v_smfmac_f32_32x32x32_bf16 v[16:31], v[184:187], v[120:127], v152\n\t" \
    "v_exp_f32 v132, v132\n\t" \
    "v_exp_f32 v133, v133\n\t" \
    "v_exp_f32 v134, v134\n\t" \
    "v_exp_f32 v135, v135\n\t" \
    "v_exp_f32 v136, v136\n\t" \
    "v_exp_f32 v137, v137\n\t" \
    "v_smfmac_f32_32x32x32_bf16 v[32:47], v[180:183], v[128:135], v152\n\t" \
    "v_exp_f32 v138, v138\n\t" \
    "v_exp_f32 v139, v139\n\t" \
    "v_exp_f32 v140, v140\n\t" \
    "v_exp_f32 v141, v141\n\t" \
    "v_mfma_f32_32x32x16_bf16 v[96:111], v[164:167], v[148:151], v[64:79]\n\t" \
    "v_exp_f32 v142, v142\n\t" \
    "v_exp_f32 v143, v143\n\t" \
    "v_exp_f32 v80, v80\n\t" \
    "v_exp_f32 v81, v81\n\t" \
    "v_exp_f32 v82, v82\n\t" \
    "v_exp_f32 v83, v83\n\t" \
    "v_smfmac_f32_32x32x32_bf16 v[32:47], v[184:187], v[136:143], v152\n\t" \
    "s_waitcnt vmcnt(3)\n\t" \
    "v_exp_f32 v84, v84\n\t" \
    "v_exp_f32 v85, v85\n\t" \
    "v_exp_f32 v86, v86\n\t" \
    "v_exp_f32 v87, v87\n\t" \
    "v_exp_f32 v88, v88\n\t" \
    "v_exp_f32 v89, v89\n\t" \
    "v_smfmac_f32_32x32x32_bf16 v[16:31], v[188:191], v[80:87], v152\n\t" \
    "v_exp_f32 v90, v90\n\t" \
    "v_exp_f32 v91, v91\n\t" \
    "v_exp_f32 v92, v92\n\t" \
    "v_exp_f32 v93, v93\n\t" \
    "v_mfma_f32_32x32x16_bf16 v[112:127], v[168:171], v[144:147], v[48:63]\n\t" \
    "v_exp_f32 v94, v94\n\t" \
    "v_exp_f32 v95, v95\n\t" \
    "v_exp_f32 v96, v96\n\t" \
    "v_exp_f32 v97, v97\n\t" \
    "v_exp_f32 v98, v98\n\t" \
    "v_exp_f32 v99, v99\n\t" \
    "v_smfmac_f32_32x32x32_bf16 v[16:31], v[192:195], v[88:95], v152\n\t" \
    "v_exp_f32 v100, v100\n\t" \
    "v_exp_f32 v101, v101\n\t" \
    "v_exp_f32 v102, v102\n\t" \
    "v_exp_f32 v103, v103\n\t" \
    "v_exp_f32 v104, v104\n\t" \
    "v_exp_f32 v105, v105\n\t" \
    "v_smfmac_f32_32x32x32_bf16 v[32:47], v[188:191], v[96:103], v152\n\t" \
    "v_exp_f32 v106, v106\n\t" \
    "v_exp_f32 v107, v107\n\t" \
    "v_exp_f32 v108, v108\n\t" \
    "v_exp_f32 v109, v109\n\t" \
    "v_mfma_f32_32x32x16_bf16 v[128:143], v[168:171], v[148:151], v[64:79]\n\t" \
    "v_exp_f32 v110, v110\n\t" \
    "v_exp_f32 v111, v111\n\t" \
    "v_exp_f32 v112, v112\n\t" \
    "v_exp_f32 v113, v113\n\t" \
    "v_exp_f32 v114, v114\n\t" \
    "v_exp_f32 v115, v115\n\t" \
    "v_smfmac_f32_32x32x32_bf16 v[32:47], v[192:195], v[104:111], v152\n\t" \
    "s_waitcnt vmcnt(0)\n\t" \
    "v_exp_f32 v116, v116\n\t" \
    "v_exp_f32 v117, v117\n\t" \
    "v_exp_f32 v118, v118\n\t" \
    "v_exp_f32 v119, v119\n\t" \
    "v_exp_f32 v120, v120\n\t" \
    "v_exp_f32 v121, v121\n\t" \
    "v_smfmac_f32_32x32x32_bf16 v[16:31], v[196:199], v[112:119], v152\n\t" \
    "v_exp_f32 v122, v122\n\t" \
    "v_exp_f32 v123, v123\n\t" \
    "v_exp_f32 v124, v124\n\t" \
    "v_exp_f32 v125, v125\n\t" \
    "v_exp_f32 v126, v126\n\t" \
    "v_exp_f32 v127, v127\n\t" \
    "v_exp_f32 v128, v128\n\t" \
    "v_exp_f32 v129, v129\n\t" \
    "v_exp_f32 v130, v130\n\t" \
    "v_exp_f32 v131, v131\n\t" \
    "v_smfmac_f32_32x32x32_bf16 v[16:31], v[200:203], v[120:127], v152\n\t" \
    "v_exp_f32 v132, v132\n\t" \
    "v_exp_f32 v133, v133\n\t" \
    "v_exp_f32 v134, v134\n\t" \
    "v_exp_f32 v135, v135\n\t" \
    "v_exp_f32 v136, v136\n\t" \
    "v_exp_f32 v137, v137\n\t" \
    "v_smfmac_f32_32x32x32_bf16 v[32:47], v[196:199], v[128:135], v152\n\t" \
    "v_exp_f32 v138, v138\n\t" \
    "v_exp_f32 v139, v139\n\t" \
    "v_exp_f32 v140, v140\n\t" \
    "v_exp_f32 v141, v141\n\t" \
    "v_exp_f32 v142, v142\n\t" \
    "v_exp_f32 v143, v143\n\t" \
    "s_nop 1\n\t" \
    "v_smfmac_f32_32x32x32_bf16 v[32:47], v[200:203], v[136:143], v152\n\t" \
    "s_waitcnt vmcnt(0)\n\t" \
    "s_mov_b32 m0, s48\n\t" \
    "s_nop 7\n\t" \
    "s_nop 3\n\t" \
    ""
#define ATTN_LOOP_CLOB "v80", "v81", "v82", "v83", "v84", "v85", "v86", "v87", "v88", "v89", "v90", "v91", "v92", "v93", "v94", "v95", "v96", "v97", "v98", "v99", "v100", "v101", "v102", "v103", "v104", "v105", "v106", "v107", "v108", "v109", "v110", "v111", "v112", "v113", "v114", "v115", "v116", "v117", "v118", "v119", "v120", "v121", "v122", "v123", "v124", "v125", "v126", "v127", "v128", "v129", "v130", "v131", "v132", "v133", "v134", "v135", "v136", "v137", "v138", "v139", "v140", "v141", "v142", "v143", "v156", "v157", "v158", "v159", "v160", "v161", "v162", "v163", "v164", "v165", "v166", "v167", "v168", "v169", "v170", "v171", "s48", "scc"
#define ATTN_LOOP_OUTS(o0,o1,n,g,b0,b1,pb,v) "+{v[16:31]}"(o0), "+{v[32:47]}"(o1), [n] "+s"(n), [g] "+s"(g), "+{s[40:41]}"(b0), "+{s[42:43]}"(b1), "+{s[46:47]}"(pb), "+{v[172:187]}"(v[0]), "+{v[188:203]}"(v[1])
#define ATTN_LOOP_INS(q0,q1,nm0,nm1,ix,ko,vo,po,kv) "{v[144:147]}"(q0), "{v[148:151]}"(q1), "{v[48:63]}"(nm0), "{v[64:79]}"(nm1), "{v152}"(ix), "{v153}"(ko), "{v154}"(vo), "{v155}"(po), "{s[44:45]}"(kv)
#define ATTN_LOOP_PIN(o0,o1,v,q0,q1,nm0,nm1,ix,ko,vo,po) asm volatile("" : "+{v[16:31]}"(o0), "+{v[32:47]}"(o1), "+{v[172:187]}"(v[0]), "+{v[188:203]}"(v[1]), "+{v[144:147]}"(q0), "+{v[148:151]}"(q1), "+{v[48:63]}"(nm0), "+{v[64:79]}"(nm1), "+{v152}"(ix), "+{v153}"(ko), "+{v154}"(vo), "+{v155}"(po))

#define LANE_CONSTS                                                                                                   \
    int tid_ = threadIdx.x; asm volatile("" : "+v"(tid_));                                                            \
    const int tid = tid_, lane = tid & 63, r = lane & 31, hh = lane >> 5;                                             \
    const int q4 = (lane & 15) >> 2, blk = (lane >> 4) & 1, p4 = lane & 3;                                            \
    const unsigned l16 = (unsigned)lane * 16;                                                                         \
          \
    const unsigned sw32 = (unsigned)((((lane >> 1) & 3) ^ blk) * 32 + (lane & 1) * 16);                               \
    const unsigned sw64 = (unsigned)(((lane & 7) ^ (blk << 2)) * 16);                                                 \
    (void)r; (void)hh; (void)q4; (void)p4; (void)l16; (void)sw32; (void)sw64

__global__ void __launch_bounds__(512, 2)
k_block(const u16* __restrict__ x, const u16* __restrict__ wqkv, const u16* __restrict__ wo, const u16* __restrict__ wg,
        const u16* __restrict__ wu, const u16* __restrict__ wd, u16* __restrict__ out, char* ws) {
    extern __shared__ __attribute__((aligned(16))) char smem[];
    STAMP_DECL; STAMP(0);
    const int wid = uni(threadIdx.x >> 6);
    const int b = blockIdx.x, grp = b & 7, idx = b >> 3;
    const unsigned lds0 = (unsigned)(uintptr_t)smem;
    const unsigned ONES = 0x3f803f80u;
    const unsigned epoch = *(const __attribute__((address_space(4))) unsigned*)(uintptr_t)(ws + WS_EPOCH) + 1u;
    const unsigned tag0 = epoch, tag1 = epoch * 0x9E3779B1u + 0x7F4A7C15u;
    const u64 tag = ((u64)tag1 << 32) | tag0;
    constexpr int L_UP = 65536, L_SCR = 98304, L_WO = 131072;
    const int w4 = wid & 3;

    {
        LANE_CONSTS;
        constexpr int L_CSP = L_WO, L_ST = L_CSP + 2048;
        if (wid >= 4) {
            const int rot = idx + grp;
            const char* xsrc = (const char*)x + (size_t)(idx * 64 + w4 * 16) * 1024;
            const char* wsrc = (const char*)wqkv + (size_t)(w4 * 16) * 3072 + grp * 128;
            const unsigned xoff = (unsigned)((lane >> 3) * 1024 + (((lane & 7) ^ (lane >> 4)) * 16));
            const unsigned qoff = (unsigned)(lane >> 3) * 3072 + sw32, voff = (unsigned)(lane >> 3) * 3072 + sw64;
            auto stage = [&](int c) {
                if (ABL == 2 && c >= 2) return;
                const int cc = (c + rot) & 7;
                const unsigned dst = lds0 + (unsigned)((c & 3) * 32768 + w4 * 2048);
                const char* wp = wsrc + (size_t)cc * 64 * 3072;
#pragma unroll
                for (int i = 0; i < 2; ++i) {
                    glds16((ABL == 4 ? wp : xsrc + cc * 128) + i * 8192, xoff ^ (unsigned)(i * 64), dst + i * 1024);
                    glds16(wp + i * 8 * 3072, qoff, dst + 8192 + i * 1024);
                    glds16(wp + i * 8 * 3072 + 1024, qoff ^ 64u, dst + 16384 + i * 1024);
                    glds16(wp + i * 8 * 3072 + 2048, voff, dst + 24576 + i * 1024);
                }
            };
            stage(0); stage(1);
            u32x4 xrow[2];
#pragma unroll
            for (int i = 0; i < 2; ++i) gload16(xrow[i], (const char*)x + (size_t)(idx * 64 + grp * 8 + w4 * 2 + i) * 1024, l16);
            WAITV(10); BARRIER();
            stage(2); stage(3);
            WAITV(16); BARRIER(); stage(4);
#pragma unroll
            for (int i = 0; i < 2; ++i) {
                PIN(xrow[i]);
                float v[8];
#pragma unroll
                for (int j = 0; j < 4; ++j) { v[2 * j] = bf_lo(xrow[i][j]); v[2 * j + 1] = bf_hi(xrow[i][j]); }
                const float mean = wave_sum(((v[0] + v[1]) + (v[2] + v[3])) + ((v[4] + v[5]) + (v[6] + v[7]))) * (1.f / D_);
                float q = 0.f;
#pragma unroll
                for (int j = 0; j < 8; ++j) { const float d = v[j] - mean; q += d * d; }
                const float rstd = __builtin_amdgcn_rsqf(wave_sum(q) * (1.f / D_) + LN_EPS);
                if (lane == 0) gstore16_sc1(ws + WS_XS + (size_t)(idx * 64 + grp * 8 + w4 * 2 + i) * 16, 0u,
                                            (u32x4){__float_as_uint(mean), tag0, __float_as_uint(rstd), tag1});
            }
#pragma unroll 1
            for (int c = 1; c < 4; ++c) { WAITV(16); BARRIER(); stage(c + 4); }
            {
                const unsigned junk = lds0 + L_WO + 24576, to = (unsigned)(lane < 14 ? lane : 0) * 128;
                const size_t u = (size_t)(b * 4 + w4) * 1792;
                touch((const char*)wg + u, to, junk); touch((const char*)wu + u, to, junk); touch((const char*)wd + u, to, junk);
            }
            WAITV(19); BARRIER();
            WAITV(11); BARRIER();
            WAITV(3); BARRIER();
            BARRIER();
        } else {
            const int tt = wid & 1, hp = wid >> 1;
            {
                u64 pc; unsigned len;
                asm volatile("s_getpc_b64 %0\n.Lcode_here:\n\ts_mov_b32 %1, .Lcode_end-.Lcode_here" : "=s"(pc), "=s"(len));
                const unsigned o = (unsigned)((idx * 4 + wid) * 4 + (lane & 3)) * 128;
                touch((const char*)pc, o < len ? o : 0u, lds0 + L_WO + 24576);
            }
            const int xr = tt * 32 + r;
            lds_cptr xp[4];
#pragma unroll
            for (int p = 0; p < 4; ++p) xp[p] = (lds_cptr)smem + xr * 128 + (((2 * (p ^ tt) + hh) ^ ((xr >> 1) & 7)) * 16);
            const int wrow = (8 * hh + q4) * 128 + p4 * 8, f1 = (q4 >> 1) & 1;
            lds_cptr ap[2][2], bp[2];
#pragma unroll
            for (int p = 0; p < 2; ++p) {
#pragma unroll
                for (int j = 0; j < 2; ++j) ap[j][p] = (lds_cptr)smem + 8192 + blk * 8192 + (p ^ tt) * 2048 + wrow + (((2 * hp + j) ^ f1 ^ (blk << 1)) * 32);
                bp[p] = (lds_cptr)smem + 24576 + (p ^ tt) * 2048 + wrow + ((hp ^ f1) * 64) + blk * 32;
            }
            const unsigned so = ((lane & 15) == blk) ? ONES : 0u;
            const bf16x8 selb = __builtin_bit_cast(bf16x8, (u32x4){so, so, so, so});
            STAMP(1);
            f32x16 aqk[2] = {}, accv = {};
            f32x4 csa[2] = {}, csb = {};
            bf16x8 xf[2][4], wa[2][2][4], wb[2][4];
            u32x4 xst;
            auto rd = [&](int c, int p, int f) {
                const int o = (c & 3) * 32768, o2 = o + (p >> 1) * 4096;
                if (f == 0) xf[c & 1][p] = lds_b128(xp[p] + o);
                else if (f < 3) wa[c & 1][f - 1][p] = cat8(lds_tr(ap[f - 1][p & 1] + o2), lds_tr(ap[f - 1][p & 1] + o2 + 512));
                else wb[c & 1][p] = cat8(lds_tr(bp[p & 1] + o2), lds_tr(bp[p & 1] + o2 + 512));
            };
            BARRIER();
            STAMP(2);
#pragma unroll
            for (int i = 0; i < 16; ++i) rd(0, i >> 2, i & 3);
#pragma unroll
            for (int c = 0; c < 8; ++c) {
                if (c < 7) BARRIER();
                if (c == 6) gload16_sc1(xst, ws + WS_XS + (size_t)(idx * 64 + tt * 32) * 16, (unsigned)r * 16);
                SBAR();
#pragma unroll
                for (int i = 0; i < (ABL == 1 ? 0 : 12); ++i) {
                    const int p = i / 3, t = i % 3;
                    if (t < 2) aqk[t] = MFMA32(wa[c & 1][t][p], xf[c & 1][p], aqk[t]);
                    else accv = MFMA32(xf[c & 1][p], wb[c & 1][p], accv);
                    SBAR();
                    if (c < 7 && i < 8) { rd(c + 1, i >> 1, (i & 1) * 2); rd(c + 1, i >> 1, (i & 1) * 2 + 1); }
                    SBAR();
                }
#pragma unroll
                for (int p = 0; p < (ABL == 1 || ABL == 3 ? 0 : 4); p += 2) {
                    csa[0] = MFMA16(wa[c & 1][0][p], selb, csa[0]); csa[1] = MFMA16(wa[c & 1][1][p], selb, csa[1]);
                    csb = MFMA16(wb[c & 1][p], selb, csb);
                }
                SBAR();
            }
            STAMP(3);
            if ((lane & 15) < 2) {
                LAS float* cp = (LAS float*)(smem + L_CSP) + tt * 192 + (lane & 15) * 16 + (lane >> 4) * 4;
                *(LAS f32x4*)(cp + hp * 64) = csa[0]; *(LAS f32x4*)(cp + hp * 64 + 32) = csa[1]; *(LAS f32x4*)(cp + 128 + hp * 32) = csb;
            }
            WAITV(0); PIN(xst);
            for (int spins = 0; spins < SPIN_CAP && !__all(xst[1] == tag0 && xst[3] == tag1); ++spins) {
                __builtin_amdgcn_s_sleep(1);
                gload16_sc1(xst, ws + WS_XS + (size_t)(idx * 64 + tt * 32) * 16, (unsigned)r * 16);
                WAITV(0); PIN(xst);
            }
            const float mean1 = __uint_as_float(xst[0]), rstd1 = __uint_as_float(xst[2]);
            LAS float* st = (LAS float*)(smem + L_ST) + wid * 64;
            st[lane] = hh ? rstd1 : mean1;
            BARRIER();
            STAMP(4);
            const LAS float* cs = (const LAS float*)(smem + L_CSP);
            const float rq = rstd1 * QSCALE;
            const int key = (r & 19) | ((r & 4) << 1) | ((r & 8) >> 1);
            char* kvg = ws + WS_KV + ((size_t)(grp * 4 + 2 * hp) * 64 + idx * 2 + tt) * 2048;
            u32x4 oq[2], ok[2];
#pragma unroll
            for (int j = 0; j < 2; ++j) {
#pragma unroll
                for (int g4 = 0; g4 < 2; ++g4) {
                    const int o = (2 * hp + j) * 32 + 8 * g4 + 4 * hh;
                    const f32x4 cq = *(const LAS f32x4*)(cs + o) + *(const LAS f32x4*)(cs + 192 + o);
                    const f32x4 ck = *(const LAS f32x4*)(cs + o + 16) + *(const LAS f32x4*)(cs + 192 + o + 16);
                    float vq[4], vk[4];
#pragma unroll
                    for (int i = 0; i < 4; ++i) {
                        vq[i] = (aqk[j][4 * g4 + i] - mean1 * cq[i]) * rq;
                        vk[i] = (aqk[j][8 + 4 * g4 + i] - mean1 * ck[i]) * rstd1;
                    }
                    oq[j][2 * g4] = cvtpk(vq[0], vq[1]); oq[j][2 * g4 + 1] = cvtpk(vq[2], vq[3]);
                    ok[j][2 * g4] = cvtpk(vk[0], vk[1]); ok[j][2 * g4 + 1] = cvtpk(vk[2], vk[3]);
                }
                gstore16_sc1(kvg + j * 131072, (unsigned)((key + 32 * hh) * 16), ok[j]);
            }
            const float csv = cs[128 + hp * 32 + r] + cs[192 + 128 + hp * 32 + r];
#pragma unroll
            for (int s = 0; s < 2; ++s) {
                u32x4 ov;
#pragma unroll
                for (int g4 = 0; g4 < 2; ++g4) {
                    const f32x4 mm = *(const LAS f32x4*)(st + 16 * s + 8 * g4 + 4 * hh);
                    const f32x4 rr = *(const LAS f32x4*)(st + 32 + 16 * s + 8 * g4 + 4 * hh);
                    float vv[4];
#pragma unroll
                    for (int i = 0; i < 4; ++i) vv[i] = (accv[8 * s + 4 * g4 + i] - mm[i] * csv) * rr[i];
                    ov[2 * g4] = cvtpk(vv[0], vv[1]); ov[2 * g4 + 1] = cvtpk(vv[2], vv[3]);
                }
                gstore16_sc1(kvg + 1024 + s * 512, (unsigned)(blk * 131072 + hh * 256 + (r & 15) * 16), ov);
            }
#pragma unroll
            for (int j = 0; j < 2; ++j) {
                *(LAS u32x4*)(smem + L_SCR + ((2 * hp + j) * 2 + tt) * 1024 + l16) = oq[j];
                const f32x16 s0 = MFMA32(__builtin_bit_cast(bf16x8, ok[j]), __builtin_bit_cast(bf16x8, oq[j]), (f32x16){});
                float m = s0[0];
#pragma unroll
                for (int g = 1; g < 16; ++g) m = fmaxf(m, s0[g]);
                m = max_halves(m);
                if (hh == 0) ((LAS float*)(smem + L_SCR + 8192))[((2 * hp + j) * 2 + tt) * 32 + r] = m;
            }
        }
        WAITV(0);
        BARRIER();
        if (tid == 0) set_flag(ws + WS_FKV + (size_t)(grp * 32 + idx) * 128, tag);
        STAMP(5);
    }

    const bool ffn1 = idx < 28;
    const int cb3 = idx & 15, rb3 = idx >> 4;
    const int tt = wid & 1, hl = wid >> 1;
    f32x16 o0 = {}, o1 = {};
    const char* kvb = ws + WS_KV + (size_t)((grp * 4 + hl) * 2 + tt) * 65536;
    {
        LANE_CONSTS;
        bf16x8 q0 = lds_b128((lds_cptr)smem + L_SCR + hl * 2048 + l16), q1 = lds_b128((lds_cptr)smem + L_SCR + hl * 2048 + 1024 + l16);
        const float m0 = ((const LAS float*)(smem + L_SCR + 8192))[hl * 64 + r], m1 = ((const LAS float*)(smem + L_SCR + 8192))[hl * 64 + 32 + r];
        f32x16 nm0, nm1;
#pragma unroll
        for (int g = 0; g < 16; ++g) { nm0[g] = -m0; nm1[g] = -m1; }
        const unsigned onesrow = ((r & 27) == 16) ? ONES : 0u;
        u32x16 vb[2];
#pragma unroll
        for (int i = 0; i < 2; ++i)
#pragma unroll
            for (int g = 0; g < 16; ++g) vb[i][g] = onesrow;
        unsigned vo = (unsigned)(1024 + hh * 256 + (r & 15) * 16), ko = l16, ix = 0xDDDD;
        const int fi = ffn1 ? idx : 0;
        u64 pb; unsigned po, pl; int ps;
        if (wid < 2) { pb = (u64)((const char*)wo + (size_t)wid * 256 * 1024 + cb3 * 64); po = (unsigned)((lane >> 2) * 1024 + (lane & 3) * 16); ps = 16384; pl = lds0 + L_WO + wid * 16384; }
        else if (wid < 6) { pb = (u64)((const char*)wg + (size_t)(wid - 2) * 128 * (F_ * 2) + fi * 128); po = (unsigned)(lane >> 3) * (F_ * 2) + sw32; ps = 8 * F_ * 2; pl = lds0 + (wid - 2) * 16384; }
        else { pb = (u64)((const char*)wu + (size_t)(wid - 6) * 128 * (F_ * 2) + fi * 128); po = ((unsigned)(lane >> 3) * (F_ * 2) + sw32) ^ 64u; ps = 8 * F_ * 2; pl = lds0 + L_UP + (wid - 6) * 16384; }
        pl = (unsigned)uni((int)pl); ps = uni(ps);
        pb = ((u64)(unsigned)uni((int)(pb >> 32)) << 32) | (unsigned)uni((int)pb);
        ATTN_LOOP_PIN(o0, o1, vb, q0, q1, nm0, nm1, ix, ko, vo, po);
        if (wid == 0) wait_flags(ws + WS_FKV + (size_t)grp * 4096, (unsigned)r * 128, true, tag);
        BARRIER();
        STAMP(6);
        if (YDELAY && wid >= 4) __builtin_amdgcn_s_sleep(YDELAY);
        int n = 7;
        const unsigned g0 = (unsigned)(idx & 7) * 8192;
        unsigned g = (g0 + 8192) & 0xffffu;
        u64 b0 = (u64)(kvb + 4096) + g0, b1 = (u64)(kvb + 4096) + g;
        asm volatile(ATTN_LOOP_ASM
                     : ATTN_LOOP_OUTS(o0, o1, n, g, b0, b1, pb, vb)
                     : ATTN_LOOP_INS(q0, q1, nm0, nm1, ix, ko, vo, po, (u64)(kvb + 4096)), [em] "s"(0x0000ffff0000ffffull),
                       [ps] "s"(ps), [pl] "s"(pl)
                     : "memory", ATTN_LOOP_CLOB);
    }
    {
        LANE_CONSTS;
        float m0 = ((const LAS float*)(smem + L_SCR + 8192))[hl * 64 + r], m1 = ((const LAS float*)(smem + L_SCR + 8192))[hl * 64 + 32 + r];
        bool good = true;
#pragma unroll
        for (int g = 0; g < 9; ++g) good = good && (fabsf(o0[g]) < 1e30f) && (fabsf(o1[g]) < 1e30f);
        if (__builtin_expect(!__all(good), 0)) {
            const bf16x8 q0 = lds_b128((lds_cptr)smem + L_SCR + hl * 2048 + l16), q1 = lds_b128((lds_cptr)smem + L_SCR + hl * 2048 + 1024 + l16);
            const unsigned onesrow = ((r & 27) == 16) ? ONES : 0u, vo = (unsigned)(1024 + hh * 256 + (r & 15) * 16);
            float x0 = -INFINITY, x1 = -INFINITY;
#pragma unroll 1
            for (int t = 0; t < 32; ++t) {
                bf16x8 kf; gload16_sc1(kf, kvb + t * 2048, l16); WAITV(0); PIN(kf);
                const f32x16 sa = MFMA32(kf, q0, (f32x16){}), sb = MFMA32(kf, q1, (f32x16){});
#pragma unroll
                for (int g = 0; g < 16; ++g) { x0 = fmaxf(x0, sa[g]); x1 = fmaxf(x1, sb[g]); }
            }
            m0 = max_halves(x0); m1 = max_halves(x1);
            o0 = (f32x16){}; o1 = (f32x16){};
#pragma unroll 1
            for (int t = 0; t < 32; ++t) {
                bf16x8 kf; u32x4 va, vc;
                gload16_sc1(kf, kvb + t * 2048, l16); gload16_sc1(va, kvb + t * 2048, vo); gload16_sc1(vc, kvb + t * 2048 + 512, vo);
                WAITV(0); PIN(kf); PIN(va); PIN(vc);
                if (blk) { va = (u32x4){onesrow, onesrow, onesrow, onesrow}; vc = va; }
                const f32x16 sa = MFMA32(kf, q0, (f32x16){}), sb = MFMA32(kf, q1, (f32x16){});
                f32x8 pa, pb, pc, pd;
#pragma unroll
                for (int g = 0; g < 8; ++g) {
                    pa[g] = __builtin_amdgcn_exp2f(sa[g] - m0); pb[g] = __builtin_amdgcn_exp2f(sa[8 + g] - m0);
                    pc[g] = __builtin_amdgcn_exp2f(sb[g] - m1); pd[g] = __builtin_amdgcn_exp2f(sb[8 + g] - m1);
                }
                o0 = __builtin_amdgcn_smfmac_f32_32x32x32_bf16(__builtin_bit_cast(bf16v8, va), __builtin_bit_cast(bf16v16, pa), o0, 0xDDDD, 0, 0);
                o0 = __builtin_amdgcn_smfmac_f32_32x32x32_bf16(__builtin_bit_cast(bf16v8, vc), __builtin_bit_cast(bf16v16, pb), o0, 0xDDDD, 0, 0);
                o1 = __builtin_amdgcn_smfmac_f32_32x32x32_bf16(__builtin_bit_cast(bf16v8, va), __builtin_bit_cast(bf16v16, pc), o1, 0xDDDD, 0, 0);
                o1 = __builtin_amdgcn_smfmac_f32_32x32x32_bf16(__builtin_bit_cast(bf16v8, vc), __builtin_bit_cast(bf16v16, pd), o1, 0xDDDD, 0, 0);
            }
        }
        STAMP(8);
        constexpr int L_CMB = L_SCR + 10240;
        {
            LAS char* cw = (LAS char*)smem + L_CMB + wid * 2560;
            *(LAS f32x4*)(cw + l16) = (f32x4){sel(tt, o0[0], o1[0]), sel(tt, o0[1], o1[1]), sel(tt, o0[2], o1[2]), sel(tt, o0[3], o1[3])};
            *(LAS f32x4*)(cw + 1024 + l16) = (f32x4){sel(tt, o0[4], o1[4]), sel(tt, o0[5], o1[5]), sel(tt, o0[6], o1[6]), sel(tt, o0[7], o1[7])};
            *(LAS f32x2*)(cw + 2048 + lane * 8) = (f32x2){sel(tt, o0[8], o1[8]), tt ? m0 : m1};
        }
        BARRIER();
        STAMP(9);
        {
            const LAS char* cr = (const LAS char*)smem + L_CMB + (wid ^ 1) * 2560;
            const f32x4 a = *(const LAS f32x4*)(cr + l16), c = *(const LAS f32x4*)(cr + 1024 + l16);
            const f32x2 lm = *(const LAS f32x2*)(cr + 2048 + lane * 8);
            float own[9];
#pragma unroll
            for (int g = 0; g < 9; ++g) own[g] = sel(tt, o1[g], o0[g]);
            const float mo = tt ? m1 : m0;
            const float mx = fmaxf(mo, lm[1]);
            const float fo = __builtin_amdgcn_exp2f(mo - mx), fr = __builtin_amdgcn_exp2f(lm[1] - mx);
            const float rl = __builtin_amdgcn_rcpf(own[8] * fo + lm[0] * fr);
            const float go = fo * rl, gr = fr * rl;
            u32x4 ob;
            ob[0] = cvtpk(own[0] * go + a[0] * gr, own[1] * go + a[1] * gr);
            ob[1] = cvtpk(own[2] * go + a[2] * gr, own[3] * go + a[3] * gr);
            ob[2] = cvtpk(own[4] * go + c[0] * gr, own[5] * go + c[1] * gr);
            ob[3] = cvtpk(own[6] * go + c[2] * gr, own[7] * go + c[3] * gr);
            gstore16_sc1(ws + WS_O + ((size_t)(idx * 2 + tt) * 32 + grp * 4 + hl) * 1024, l16, ob);
        }
        WAITV(0);
        BARRIER();
        if (tid == 0) set_flag(ws + WS_FO + (size_t)(idx * 8 + grp) * 128, tag);
        STAMP(10);
    }

    const int kh = wid >> 2;
    {
        LANE_CONSTS;
        const int mt3 = grp * 8 + rb3 * 4 + w4;
        u32x2 xv[4];
        if (kh == 0) {
#pragma unroll
            for (int g4 = 0; g4 < 4; ++g4) gload8(xv[g4], (const char*)x + (size_t)(mt3 * 32) * 1024 + cb3 * 64 + g4 * 16, (unsigned)(r * 1024 + hh * 8));
        } else if (ffn1) {
#pragma unroll
            for (int i = 0; i < 8; ++i)
                glds16((const char*)wu + (size_t)(256 + w4 * 64 + i * 8) * (F_ * 2) + idx * 128, ((unsigned)(lane >> 3) * (F_ * 2) + sw32) ^ 64u,
                       lds0 + (unsigned)(L_SCR + w4 * 8192 + i * 1024));
        }
        bf16x8 wf[16];
#pragma unroll
        for (int i = 0; i < 16; ++i) {
            lds_cptr wp = (lds_cptr)smem + L_WO + (kh * 16 + i) * 1024 + (4 * hh + q4) * 64 + blk * 32 + p4 * 8;
            wf[i] = cat8(lds_tr(wp), lds_tr(wp + 512));
        }
#pragma unroll
        for (int i = 0; i < 16; ++i) PIN(wf[i]);
        if (wid == 0) wait_flags(ws + WS_FO + (size_t)(grp * 4 + rb3 * 2) * 1024, (unsigned)(lane & 15) * 128, true, tag);
        BARRIER();
        STAMP(11);
        bf16x8 af[16];
#pragma unroll
        for (int i = 0; i < 16; ++i) gload16_sc1(af[i], ws + WS_O + ((size_t)mt3 * 32 + kh * 16 + i) * 1024, l16);
        f32x16 acc = {};
#pragma unroll
        for (int i = 0; i < 16; i += 2) {
            WAITV(14 - i);
#pragma unroll
            for (int j = 0; j < 2; ++j) { PIN(af[i + j]); acc = MFMA32(wf[i + j], af[i + j], acc); }
        }
        STAMP(12);
        LAS char* cw = (LAS char*)smem + L_WO + w4 * 4096 + l16;
        if (kh) {
#pragma unroll
            for (int g4 = 0; g4 < 4; ++g4) *(LAS f32x4*)(cw + g4 * 1024) = (f32x4){acc[4 * g4], acc[4 * g4 + 1], acc[4 * g4 + 2], acc[4 * g4 + 3]};
        }
        BARRIER();
        if (kh == 0) {
            u32x4 hv[2];
            float s1 = 0.f, s2 = 0.f;
#pragma unroll
            for (int g4 = 0; g4 < 4; ++g4) {
                PIN(xv[g4]);
                const f32x4 a = *(const LAS f32x4*)(cw + g4 * 1024);
                const unsigned w0 = cvtpk(acc[4 * g4] + a[0] + bf_lo(xv[g4][0]), acc[4 * g4 + 1] + a[1] + bf_hi(xv[g4][0]));
                const unsigned w1 = cvtpk(acc[4 * g4 + 2] + a[2] + bf_lo(xv[g4][1]), acc[4 * g4 + 3] + a[3] + bf_hi(xv[g4][1]));
                hv[g4 >> 1][(g4 & 1) * 2] = w0; hv[g4 >> 1][(g4 & 1) * 2 + 1] = w1;
                const float e0 = bf_lo(w0), e1 = bf_hi(w0), e2 = bf_lo(w1), e3 = bf_hi(w1);
                s1 += (e0 + e1) + (e2 + e3); s2 += (e0 * e0 + e1 * e1) + (e2 * e2 + e3 * e3);
            }
            gstore16_sc1(ws + WS_H + ((size_t)mt3 * 32 + cb3 * 2) * 1024, l16, hv[0]);
            gstore16_sc1(ws + WS_H + ((size_t)mt3 * 32 + cb3 * 2 + 1) * 1024, l16, hv[1]);
            s1 = sum_halves(s1); s2 = sum_halves(s2);
            if (hh == 0) gstore8_sc1(ws + WS_HS + ((size_t)mt3 * 8 + (cb3 >> 1)) * 512 + (cb3 & 1) * 8, (unsigned)r * 16,
                                     (u32x2){__float_as_uint(s1), __float_as_uint(s2)});
        }
        WAITV(0);
        BARRIER();
        if (tid == 0) set_flag(ws + WS_FH + (size_t)(grp * 32 + idx) * 128, tag);
        STAMP(13);
    }

    const int rb = idx >> 3, cb = idx & 7;
    if (ffn1) {
        LANE_CONSTS;
        const int sw = ((q4 >> 1) & 1) ^ (blk << 1);
        lds_cptr gp = (lds_cptr)smem + blk * L_UP + (4 * hh + q4) * 128 + p4 * 8;
        lds_cptr gpj[4];
#pragma unroll
        for (int j = 0; j < 4; ++j) gpj[j] = gp + ((j ^ sw) * 32);
        {
            float c[4] = {};
#pragma unroll
            for (int i = 0; i < 4; ++i)
#pragma unroll
                for (int j = 0; j < 4; ++j) {
                    lds_cptr wp = gpj[j] + (wid * 4 + i) * 2048;
                    const u32x4 a = __builtin_bit_cast(u32x4, cat8(lds_tr(wp), lds_tr(wp + 1024)));
                    c[j] = dot2bf(a[3], ONES, dot2bf(a[2], ONES, dot2bf(a[1], ONES, dot2bf(a[0], ONES, c[j]))));
                }
#pragma unroll
            for (int j = 0; j < 4; ++j) c[j] = sum_halves(c[j]);
            LAS float* cp = (LAS float*)(smem + L_WO) + wid * 128;
            cp[lane] = hh ? c[1] : c[0]; cp[64 + lane] = hh ? c[3] : c[2];
        }
        BARRIER();
        if (wid >= 6) {
            const LAS float* cp = (const LAS float*)(smem + L_WO) + (tid - 384);
            ((LAS float*)(smem + L_WO + 4096))[tid - 384] = ((cp[0] + cp[128]) + (cp[256] + cp[384])) + ((cp[512] + cp[640]) + (cp[768] + cp[896]));
        }
        const int mt4 = grp * 8 + wid;
        const char* hsrc = ws + WS_H + (size_t)mt4 * 32768;
        char* adst = ws + WS_A + ((size_t)mt4 * 112 + idx * 4) * 1024;
        const LAS float* cs = (const LAS float*)(smem + L_WO + 4096);
        u32x4 hs[4];
        float mean, ve, k1, k3;
        auto stats = [&]() {
            float s1 = 0.f, s2 = 0.f;
#pragma unroll
            for (int i = 0; i < 4; ++i) {
                PIN(hs[i]);
                s1 += __uint_as_float(hs[i][0]) + __uint_as_float(hs[i][2]); s2 += __uint_as_float(hs[i][1]) + __uint_as_float(hs[i][3]);
            }
            s1 = sum_halves(s1); s2 = sum_halves(s2);
            mean = s1 * (1.f / D_);
            ve = fmaxf(s2 * (1.f / D_) - mean * mean, 0.f) + LN_EPS;
            const float rstd = __builtin_amdgcn_rsqf(ve);
            k1 = GELU_C1 * rstd; k3 = GELU_C3 * rstd * rstd * rstd;
        };
        float ta[8], ya[8];
        u32x4 ob;
        auto actop = [&](const f32x16& p, int n) {
            const int st = n >> 3, v = n & 7;
            if (st == 0) ta[v] = fmul1(p[v], p[v]);
            else if (st == 1) ta[v] = ffma1(ta[v], k3, k1);
            else if (st == 2) ta[v] = fmul1(ta[v], p[v]);
            else if (st == 3) { ta[v] = __builtin_amdgcn_exp2f(ta[v]); PIN(ta[v]); }
            else if (st == 4) ya[v] = fmul1(p[v], p[8 + v]);
            else if (st == 5) ta[v] = ffma1(ta[v], ve, ve);
            else if (st == 6) { ta[v] = __builtin_amdgcn_rcpf(ta[v]); PIN(ta[v]); }
            else if (st == 7) ya[v] = fmul1(ya[v], ta[v]);
            else { unsigned w = cvtpk(ya[2 * v], ya[2 * v + 1]); PIN(w); ob[v] = w; }
        };
        if (wid < 4) {
            bf16x8 wf[2][4];
#pragma unroll
            for (int j = 0; j < 4; ++j) wf[0][j] = cat8(lds_tr(gpj[j]), lds_tr(gpj[j] + 1024));
            f32x16 acc[4];
#pragma unroll
            for (int j = 0; j < 4; ++j) { acc[j] = (f32x16){}; PIN(acc[j]); }
            if (wid == 0) wait_flags(ws + WS_FH + (size_t)grp * 4096, (unsigned)r * 128, true, tag);
            BARRIER();
            STAMP(14);
            __builtin_amdgcn_s_setprio(2);
            constexpr int AW = 8;
            bf16x8 af[32];
#pragma unroll
            for (int i = 0; i < AW; ++i) gload16_sc1(af[i], hsrc + i * 1024, l16);
#pragma unroll
            for (int ks = 0; ks < 32; ++ks) {
                WAITV(ks < 29 ? AW - 1 : 31 - ks + 4); PIN(af[ks]);
                SBAR();
#pragma unroll
                for (int j = 0; j < 4; ++j) {
                    acc[j] = MFMA32(wf[ks & 1][j], af[ks], acc[j]);
                    SBAR();
                    if (ks < 31) wf[(ks + 1) & 1][j] = cat8(lds_tr(gpj[j] + (ks + 1) * 2048), lds_tr(gpj[j] + (ks + 1) * 2048 + 1024));
                    if (j == 0) {
                        if (ks + AW < 32) gload16_sc1(af[ks + AW], hsrc + (ks + AW) * 1024, l16);
                        else if (ks + AW < 36) gload16_sc1(hs[ks + AW - 32], ws + WS_HS + (size_t)mt4 * 4096 + (ks + AW - 32) * 1024, l16);
                    }
                    SBAR();
                }
            }
            STAMP(15);
#pragma unroll
            for (int i = 0; i < 2; ++i)
                touch((const char*)wd + (size_t)(rb * 448 + wid * 112 + i * 56) * 1024 + cb * 128, (unsigned)(lane < 56 ? lane : 0) * 1024, lds0 + L_WO + 24576);
            __builtin_amdgcn_s_setprio(0);
            WAITV(2);
            stats();
#pragma unroll
            for (int j = 0; j < 4; ++j) {
#pragma unroll
                for (int g4 = 0; g4 < 4; ++g4) {
                    const f32x4 c4 = *(const LAS f32x4*)(cs + j * 32 + 8 * g4 + 4 * hh);
#pragma unroll
                    for (int i = 0; i < 4; ++i) acc[j][4 * g4 + i] = ffma1(-mean, c4[i], acc[j][4 * g4 + i]);
                }
#pragma unroll
                for (int n = 0; n < 68; ++n) actop(acc[j], n);
                gstore16_sc1(adst + j * 1024, l16, ob);
            }
        } else {
            bf16x8 wf[8];
#pragma unroll
            for (int k = 0; k < 6; ++k) wf[k] = cat8(lds_tr(gpj[0] + k * 2048), lds_tr(gpj[0] + k * 2048 + 1024));
            BARRIER();
            STAMP(14);
            __builtin_amdgcn_s_setprio(1);
            __builtin_amdgcn_s_sleep(4);
            bf16x8 af[32];
#pragma unroll
            for (int i = 0; i < 4; ++i) gload16_sc1(hs[i], ws + WS_HS + (size_t)mt4 * 4096 + i * 1024, l16);
#pragma unroll
            for (int i = 0; i < 32; ++i) gload16_sc1(af[i], hsrc + i * 1024, l16);
            WAITV(32);
            stats();
            f32x16 acc[2];
            f32x4 c4;
#pragma unroll
            for (int g4 = 0; g4 < 4; ++g4) {
                c4 = *(const LAS f32x4*)(cs + 8 * g4 + 4 * hh);
#pragma unroll
                for (int i = 0; i < 4; ++i) acc[0][4 * g4 + i] = fmul1(c4[i], -mean);
            }
#pragma unroll
            for (int t = 0; t < 4; ++t) {
#pragma unroll
                for (int ks = 0; ks < 32; ++ks) {
                    if (t == 0 && (ks & 3) == 0) { WAITV(28 - ks); PIN(af[ks]); PIN(af[ks + 1]); PIN(af[ks + 2]); PIN(af[ks + 3]); }
                    SBAR();
                    acc[t & 1] = MFMA32(wf[ks & 7], af[ks], acc[t & 1]);
                    SBAR();
                    const int nk = (ks + 6) & 31, nt = t + (ks >= 26 ? 1 : 0);
                    if (nt < 4) wf[(ks + 6) & 7] = cat8(lds_tr(gpj[nt & 3] + nk * 2048), lds_tr(gpj[nt & 3] + nk * 2048 + 1024));
                    if (t > 0 && ks >= 1 && ks < 24) {
#pragma unroll
                        for (int n = 3 * (ks - 1); n < 3 * ks && n < 68; ++n) actop(acc[(t - 1) & 1], n);
                        if (ks == 23) gstore16_sc1(adst + (t - 1) * 1024, l16, ob);
                    }
                    if (t < 3 && ks >= 22 && ks < 31 && (ks & 1) == 0) {
                        const int g4 = (ks - 22) / 2 - 1;
                        if (g4 >= 0) {
#pragma unroll
                            for (int i = 0; i < 4; ++i) acc[(t + 1) & 1][4 * g4 + i] = fmul1(c4[i], -mean);
                        }
                        if (g4 < 3) c4 = *(const LAS f32x4*)(cs + (t + 1) * 32 + 8 * (g4 + 1) + 4 * hh);
                    }
                    SBAR();
                }
            }
            STAMP(15);
#pragma unroll
            for (int n = 0; n < 68; ++n) actop(acc[1], n);
            gstore16_sc1(adst + 3 * 1024, l16, ob);
            __builtin_amdgcn_s_setprio(0);
        }
        STAMP(16);
        WAITV(0);
        BARRIER();
        if (tid == 0) set_flag(ws + WS_FA + (size_t)(grp * 32 + idx) * 128, tag);
    }
    STAMP(17);

    {
        LANE_CONSTS;
        const int wm = wid & 1, kq = wid >> 1;
        const int mt5 = grp * 8 + 2 * rb + wm;
        const unsigned woff = (unsigned)(lane >> 3) * 1024 + sw64;
        const char* wsrc = (const char*)wd + (size_t)(kq * 448 + wm * 8) * 1024 + cb * 128;
        const unsigned wdst = lds0 + (unsigned)(kq * 40960 + wm * 1024);
#pragma unroll 4
        for (int i = 0; i < 20; ++i) glds16(wsrc + (size_t)i * 16384, woff, wdst + (unsigned)(i * 2048));
        lds_cptr wp[2];
#pragma unroll
        for (int j = 0; j < 2; ++j)
            wp[j] = (lds_cptr)smem + kq * 40960 + (4 * hh + q4) * 128 + ((j ^ (kq >> 1) ^ ((q4 >> 1) & 1)) * 64) + (blk ^ (kq & 1)) * 32 + p4 * 8;
        WAITV(12);
        BARRIER();
        bf16x8 wr[8][2];
#pragma unroll
        for (int i = 0; i < 8; ++i)
#pragma unroll
            for (int j = 0; j < 2; ++j) wr[i][j] = cat8(lds_tr(wp[j] + i * 2048), lds_tr(wp[j] + i * 2048 + 1024));
#pragma unroll
        for (int i = 0; i < 8; ++i) { PIN(wr[i][0]); PIN(wr[i][1]); }
        BARRIER();
        auto stage_c = [&]() {
#pragma unroll
            for (int i = 0; i < 8; ++i) glds16(wsrc + (size_t)(20 + i) * 16384, woff, wdst + (unsigned)(i * 2048));
        };
        if (wid == 0) wait_flags(ws + WS_FA + (size_t)grp * 4096, (unsigned)r * 128, r < 28, tag);
        else { stage_c(); WAITV(8); }
        BARRIER();
        STAMP(18);
        if (wid == 0) stage_c();
        const char* asrc = ws + WS_A + ((size_t)mt5 * 112 + kq * 28) * 1024;
        f32x16 acc[2] = {};
        u32x4 hreg;
        const int n5 = cb * 64 + kq * 16;
        auto body = [&](auto awc) {
            constexpr int AW = decltype(awc)::value;
            bf16x8 af[28], wf[4][2];
#pragma unroll
            for (int i = 0; i < AW; ++i) gload16_sc1(af[i], asrc + i * 1024, l16);
#pragma unroll
            for (int ks = 0; ks < 28; ++ks) {
                WAITV(ks <= 28 - AW + 1 && !(AW == 28 && ks > 0) ? AW - 1 : 28 - ks);
                PIN(af[ks]);
                if (ks == 17) BARRIER();
                SBAR();
#pragma unroll
                for (int j = 0; j < 2; ++j) {
                    acc[j] = MFMA32(ks < 8 ? wr[ks < 8 ? ks : 0][j] : wf[ks & 3][j], af[ks], acc[j]);
                    SBAR();
                    if (ks >= 5 && ks < 25) {
                        const int o = (ks + 3 < 20 ? ks + 3 : ks + 3 - 20) * 2048;
                        wf[(ks + 3) & 3][j] = cat8(lds_tr(wp[j] + o), lds_tr(wp[j] + o + 1024));
                    }
                    if (j == 0 && ks + AW < 28) gload16_sc1(af[ks + AW], asrc + (ks + AW) * 1024, l16);
                    if (j == 0 && ks + AW == 28) gload16_sc1(hreg, ws + WS_H + ((size_t)mt5 * 32 + n5 / 16) * 1024, l16);
                    SBAR();
                }
            }
        };
        body(IntC<P5AW>{});
        STAMP(19);
#pragma unroll
        for (int i = 1; i < 4; ++i) {
            LAS char* cw = (LAS char*)smem + (kq ^ i) * 40960 + 16384 + wm * 8192 + i * 2048 + l16;
            const f32x16& a = acc[i >> 1];
            const int o = (i & 1) * 8;
            *(LAS f32x4*)cw = (f32x4){a[o], a[o + 1], a[o + 2], a[o + 3]};
            *(LAS f32x4*)(cw + 1024) = (f32x4){a[o + 4], a[o + 5], a[o + 6], a[o + 7]};
        }
        WAITV(0);
        BARRIER();
        f32x4 lo = {acc[0][0], acc[0][1], acc[0][2], acc[0][3]}, hi = {acc[0][4], acc[0][5], acc[0][6], acc[0][7]};
#pragma unroll
        for (int i = 1; i < 4; ++i) {
            const LAS char* cr = (const LAS char*)smem + kq * 40960 + 16384 + wm * 8192 + i * 2048 + l16;
            lo += *(const LAS f32x4*)cr; hi += *(const LAS f32x4*)(cr + 1024);
        }
        PIN(hreg);
        const unsigned w0 = cvtpk(lo[0] + bf_lo(hreg[0]), lo[1] + bf_hi(hreg[0])), w1 = cvtpk(lo[2] + bf_lo(hreg[1]), lo[3] + bf_hi(hreg[1]));
        const unsigned w2 = cvtpk(hi[0] + bf_lo(hreg[2]), hi[1] + bf_hi(hreg[2])), w3 = cvtpk(hi[2] + bf_lo(hreg[3]), hi[3] + bf_hi(hreg[3]));
        const auto s0 = __builtin_amdgcn_permlane32_swap(w0, w2, false, false);
        const auto s1 = __builtin_amdgcn_permlane32_swap(w1, w3, false, false);
        *(u32x4*)(out + (size_t)(mt5 * 32 + r) * D_ + n5 + hh * 8) = (u32x4){s0[0], s1[0], s0[1], s1[1]};
        if (b == 0 && tid == 0) *(unsigned*)(ws + WS_EPOCH) = epoch;
    }
    STAMP(20);
    STAMP_OUT();
    asm volatile(".Lcode_end:");
}

extern "C" void kernel_launch(void* const* d_in, const int* in_sizes, int n_in,
                              void* d_out, int out_size, void* d_ws, size_t ws_size,
                              hipStream_t stream) {
    (void)hipFuncSetAttribute((const void*)k_block, hipFuncAttributeMaxDynamicSharedMemorySize, LDS_BYTES);
    k_block<<<256, 512, LDS_BYTES, stream>>>((const u16*)d_in[0], (const u16*)d_in[1], (const u16*)d_in[2], (const u16*)d_in[4],
                                             (const u16*)d_in[5], (const u16*)d_in[6], (u16*)d_out, (char*)d_ws);
}
```
